```python
import torch

def _softmax(x, axis=-1):
    return torch.softmax(x, dim=axis)
def _gelu(x):
    return torch.nn.functional.gelu(x, approximate="tanh")
def _silu(x):
    return torch.nn.functional.silu(x)
def _sigmoid(x):
    return torch.sigmoid(x)
def _layernorm(x, axis=-1, eps=1e-5):
    m = x.mean(dim=axis, keepdim=True)
    v = x.var(dim=axis, keepdim=True, unbiased=False)
    return (x - m) / torch.sqrt(v + eps)
def _scatter_add(tgt, idx, src, axis=0):
    return tgt.clone().index_add_(axis, idx, src)
def _segment_sum(src, seg, n):
    out = torch.zeros((n,) + src.shape[1:], dtype=src.dtype)
    return out.index_add_(0, seg, src)
def _pad(x, pw):
    flat = tuple(v for pair in reversed(pw) for v in pair)
    return torch.nn.functional.pad(x, flat)

kernel_name = 'gated_mix__linear_kernel_hedgehog__geglu_ffn__prompt'

def setup_inputs():

    _w = (lambda a: (lambda t: t.to(torch.bfloat16) if t.is_floating_point() else t)(torch.as_tensor(a)))
    def _A_setup():
        B, S, D, H = 1, 2048, 640, 32
        Dh = D // H
        return {
            "x": _w((lambda *s: torch.randn(*s, dtype=torch.bfloat16) * 0.05)(B, S, D)),
            "wqkv": _w((lambda *s: torch.randn(*s, dtype=torch.bfloat16) * 0.8)(D, 3 * D)),
            "w_phi": _w((lambda *s: torch.randn(*s, dtype=torch.bfloat16) * 0.8)(H, Dh, Dh) * (Dh ** -0.5)),
            "wo": _w((lambda *s: torch.randn(*s, dtype=torch.bfloat16) * 0.05)(D, D)),
        }
    _A_in = _A_setup()

    def _B_setup():
        B, S, D, F = 1, 2048, 640, 1728
        return {
            "x": _w((lambda *s: torch.randn(*s, dtype=torch.bfloat16) * 0.05)(B, S, D)),
            "w_gate": _w((lambda *s: torch.randn(*s, dtype=torch.bfloat16) * 0.05)(D, F)),
            "w_up": _w((lambda *s: torch.randn(*s, dtype=torch.bfloat16) * 0.05)(D, F)),
            "w_down": _w((lambda *s: torch.randn(*s, dtype=torch.bfloat16) * 0.05)(F, D)),
        }
    _B_in = _B_setup()

    return {
        "A_x": _A_in["x"],
        "A_wqkv": _A_in["wqkv"],
        "A_w_phi": _A_in["w_phi"],
        "A_wo": _A_in["wo"],
        "B_x": _B_in["x"],
        "B_w_gate": _B_in["w_gate"],
        "B_w_up": _B_in["w_up"],
        "B_w_down": _B_in["w_down"],
        "gate_w": _w((lambda *s: torch.randn(*s, dtype=torch.bfloat16) * 0.05)(640) * 16.0),
    }

def reference(A_x, A_wqkv, A_w_phi, A_wo, B_x, B_w_gate, B_w_up, B_w_down, gate_w):
    def _A_ref(x, wqkv, w_phi, wo):
        B, S, D = x.shape
        H = 32; Dh = D // H
        qkv = (lambda x, *d: x.permute(*d))((x @ wqkv).reshape(B, S, 3, H, Dh), 2, 0, 3, 1, 4)
        q, k, v = qkv[0], qkv[1], qkv[2]
        pq = torch.einsum("bhsd,hdf->bhsf", q, w_phi)
        pk = torch.einsum("bhsd,hdf->bhsf", k, w_phi)
        phi_q = torch.concatenate([_softmax(pq, axis=-1), _softmax(-pq, axis=-1)], axis=-1)
        phi_k = torch.concatenate([_softmax(pk, axis=-1), _softmax(-pk, axis=-1)], axis=-1)
        kv = torch.einsum("bhsf,bhsd->bhfd", phi_k, v)
        z  = torch.einsum("bhsf->bhf", phi_k)
        num = torch.einsum("bhqf,bhfd->bhqd", phi_q, kv)
        den = torch.einsum("bhqf,bhf->bhq", phi_q, z)[..., None] + 1e-6
        o = num / den
        return (lambda x, *d: x.permute(*d))(o, 0, 2, 1, 3).reshape(B, S, D) @ wo

    def _B_ref(x, w_gate, w_up, w_down):
        return (_gelu(x @ w_gate) * (x @ w_up)) @ w_down

    x = A_x
    g = _sigmoid((x * gate_w).sum(-1, keepdims=True))
    a = _A_ref(x, A_wqkv, A_w_phi, A_wo)
    b = _B_ref(x, B_w_gate, B_w_up, B_w_down)
    return g * a + (1 - g) * b
```

```cpp
#include <hip/hip_runtime.h>
#include <hip/hip_bf16.h>
typedef __hip_bfloat16 bf16;
static __device__ __forceinline__ float b2f(bf16 v){ return __bfloat162float(v); }

template<typename TA>
__global__ void gemm_naive(const TA* A, const bf16* B, float* C, int M, int N, int K){
  __shared__ float As[16][17]; __shared__ float Bs[16][17];
  int tx=threadIdx.x, ty=threadIdx.y;
  int row=blockIdx.y*16+ty, col=blockIdx.x*16+tx;
  float acc=0;
  for(int k0=0;k0<K;k0+=16){
    As[ty][tx]=(float)A[(size_t)row*K+k0+tx];
    Bs[ty][tx]=b2f(B[(size_t)(k0+ty)*N+col]);
    __syncthreads();
    for(int k=0;k<16;k++) acc+=As[ty][k]*Bs[k][tx];
    __syncthreads();
  }
  C[(size_t)row*N+col]=acc;
}
__global__ void phi_kernel(const float* qkv, const bf16* wphi, float* phi){
  int idx=blockIdx.x*blockDim.x+threadIdx.x;
  if(idx>=2048*64) return;
  int s=idx/64, w=(idx/32)&1, h=idx&31;
  const float* q=qkv+(size_t)s*1920+w*640+h*20;
  float* o=phi+((size_t)w*2048*32+(size_t)s*32+h)*40;
  float mx=-1e30f, mn=1e30f;
  for(int f=0;f<20;f++){ float a=0; for(int d=0;d<20;d++) a+=q[d]*b2f(wphi[h*400+d*20+f]); o[f]=a; mx=fmaxf(mx,a); mn=fminf(mn,a);}
  float s1=0,s2=0;
  for(int f=0;f<20;f++){ float a=o[f]; float e1=__expf(a-mx), e2=__expf(mn-a); o[f]=e1; o[20+f]=e2; s1+=e1; s2+=e2;}
  for(int f=0;f<20;f++){ o[f]/=s1; o[20+f]/=s2; }
}
__global__ void kv_kernel(const float* qkv, const float* phi, float* kv){
  int h=blockIdx.x; int t=threadIdx.x; if(t>=840) return;
  int f=t/21, d=t%21; float a=0;
  const float* pk=phi+(size_t)2048*32*40;
  for(int s=0;s<2048;s++){ float p=pk[((size_t)s*32+h)*40+f]; float v= d<20? qkv[(size_t)s*1920+1280+h*20+d]:1.f; a+=p*v; }
  kv[h*840+t]=a;
}
__global__ void o_kernel(const float* phi, const float* kv, float* o){
  int idx=blockIdx.x*blockDim.x+threadIdx.x; if(idx>=2048*640) return;
  int s=idx/640, c=idx%640, h=c/20, d=c%20;
  const float* pq=phi+((size_t)s*32+h)*40; float num=0, den=0;
  for(int f=0;f<40;f++){ num+=pq[f]*kv[h*840+f*21+d]; den+=pq[f]*kv[h*840+f*21+20]; }
  o[idx]=num/(den+1e-6f);
}
__global__ void h_kernel(const float* g, const float* u, float* h){
  int idx=blockIdx.x*blockDim.x+threadIdx.x; if(idx>=2048*1728) return;
  float x=g[idx]; float t=tanhf(0.7978845608028654f*(x+0.044715f*x*x*x));
  h[idx]=0.5f*x*(1.f+t)*u[idx];
}
__global__ void final_kernel(const bf16* x, const bf16* gw, const float* a, const float* b, bf16* out){
  int s=blockIdx.x; __shared__ float red[256];
  float acc=0; for(int c=threadIdx.x;c<640;c+=256) acc+=b2f(x[s*640+c])*b2f(gw[c]);
  red[threadIdx.x]=acc; __syncthreads();
  for(int o=128;o>0;o>>=1){ if(threadIdx.x<o) red[threadIdx.x]+=red[threadIdx.x+o]; __syncthreads(); }
  float g=1.f/(1.f+__expf(-red[0]));
  for(int c=threadIdx.x;c<640;c+=256) out[s*640+c]=__float2bfloat16(g*a[s*640+c]+(1.f-g)*b[s*640+c]);
}
extern "C" void kernel_launch(void* const* d_in, const int* in_sizes, int n_in,
                              void* d_out, int out_size, void* d_ws, size_t ws_size,
                              hipStream_t stream) {
  const bf16* x=(const bf16*)d_in[0]; const bf16* wqkv=(const bf16*)d_in[1]; const bf16* wphi=(const bf16*)d_in[2];
  const bf16* wo=(const bf16*)d_in[3]; const bf16* wg=(const bf16*)d_in[5]; const bf16* wu=(const bf16*)d_in[6];
  const bf16* wd=(const bf16*)d_in[7]; const bf16* gw=(const bf16*)d_in[8];
  float* ws=(float*)d_ws;
  float* qkv=ws; ws+=2048*1920; float* phi=ws; ws+=2*2048*32*40; float* kv=ws; ws+=32*840+32; float* o=ws; ws+=2048*640;
  float* G=ws; ws+=2048*1728; float* U=ws; ws+=2048*1728; float* H=ws; ws+=2048*1728; float* a=ws; ws+=2048*640; float* b=ws; ws+=2048*640;
  dim3 bl(16,16);
  gemm_naive<bf16><<<dim3(1920/16,128),bl,0,stream>>>(x,wqkv,qkv,2048,1920,640);
  gemm_naive<bf16><<<dim3(1728/16,128),bl,0,stream>>>(x,wg,G,2048,1728,640);
  gemm_naive<bf16><<<dim3(1728/16,128),bl,0,stream>>>(x,wu,U,2048,1728,640);
  phi_kernel<<<2048*64/256,256,0,stream>>>(qkv,wphi,phi);
  kv_kernel<<<32,896,0,stream>>>(qkv,phi,kv);
  o_kernel<<<2048*640/256,256,0,stream>>>(phi,kv,o);
  h_kernel<<<2048*1728/256,256,0,stream>>>(G,U,H);
  gemm_naive<float><<<dim3(640/16,128),bl,0,stream>>>(o,wo,a,2048,640,640);
  gemm_naive<float><<<dim3(640/16,128),bl,0,stream>>>(H,wd,b,2048,640,1728);
  final_kernel<<<2048,256,0,stream>>>(x,gw,a,b,(bf16*)d_out);
}
```

```cpp
#include <hip/hip_runtime.h>
#include <hip/hip_bf16.h>
#include <stdint.h>
#include <type_traits>
#define STAMPK(ws, kern, i) do {} while (0)
#define STAMPC(ws, kern, i) do {} while (0)

typedef __attribute__((ext_vector_type(8))) short s16x8;
typedef __attribute__((ext_vector_type(4))) short s16x4;
typedef __attribute__((ext_vector_type(8))) __bf16 bf16x8;
typedef __attribute__((ext_vector_type(2))) __bf16 bf16x2;
typedef __attribute__((ext_vector_type(4))) float f32x4;
typedef __attribute__((ext_vector_type(2))) float f32x2;
typedef __attribute__((ext_vector_type(4))) unsigned u32x4;
typedef __attribute__((ext_vector_type(2))) unsigned u32x2;
typedef unsigned short u16;
#define GAS __attribute__((address_space(1)))
#define LAS __attribute__((address_space(3)))
#define DI __device__ __forceinline__

constexpr int S_ = 2048, D_ = 640, F_ = 1728, NQKV = 1920;
constexpr int KS_H = 54, KS_O = 20, KS_T = 74;

constexpr size_t WS_HA   = 0;
constexpr size_t WS_WB   = WS_HA + (size_t)32 * KS_T * 4096;
constexpr size_t WS_PQ = WS_WB + (size_t)8 * KS_T * 5120;
constexpr size_t WS_SLAB = WS_PQ + (size_t)32 * 2048 * 48;
constexpr size_t WS_G    = WS_SLAB + (size_t)8 * 32 * 3360;
constexpr size_t WS_CNT  = WS_G + 2048 * 4;

DI f32x4 mfma16(s16x8 a, s16x8 b, f32x4 c) {
  return __builtin_amdgcn_mfma_f32_16x16x32_bf16(__builtin_bit_cast(bf16x8, a), __builtin_bit_cast(bf16x8, b), c, 0, 0, 0);
}
DI unsigned pack2(float a, float b) {
  bf16x2 v = {(__bf16)a, (__bf16)b};
  return __builtin_bit_cast(unsigned, v);
}
DI s16x4 tr_read(const char* lds) {
  return __builtin_amdgcn_ds_read_tr16_b64_v4i16((LAS s16x4*)(lds));
}
DI s16x8 cat(s16x4 a, s16x4 b) { return __builtin_shufflevector(a, b, 0, 1, 2, 3, 4, 5, 6, 7); }
DI void glds16(const void* sbase, unsigned voff, unsigned ldsaddr) {
  asm volatile("s_mov_b32 m0, %2\n\ts_nop 0\n\tglobal_load_lds_dwordx4 %0, %1" :: "v"(voff), "s"(sbase), "s"(ldsaddr) : "memory");
}
DI void glds16m(const void* sbase, unsigned voff, unsigned ldsaddr, unsigned long long mask) {
  asm volatile("s_mov_b64 exec, %3\n\ts_mov_b32 m0, %2\n\ts_nop 0\n\tglobal_load_lds_dwordx4 %0, %1\n\ts_mov_b64 exec, -1"
               :: "v"(voff), "s"(sbase), "s"(ldsaddr), "s"(mask) : "memory");
}
DI unsigned lds_addr(const char* p) { return (unsigned)(size_t)(const LAS char*)p; }
DI float fexp2(float x) { return __builtin_amdgcn_exp2f(x); }
DI float frcp(float x) { return __builtin_amdgcn_rcpf(x); }
#define WAIT_V(n) asm volatile("s_waitcnt vmcnt(%0)" :: "n"(n) : "memory")
#define WAIT_L0() asm volatile("s_waitcnt lgkmcnt(0)" ::: "memory")
#define BAR() __builtin_amdgcn_s_barrier()
#define SCHED() __builtin_amdgcn_sched_barrier(0)

struct P1 {
  const u16* x; const u16* wqkv; const u16* wphi; const u16* wo;
  const u16* wg; const u16* wu; const u16* wd; const u16* gw;
  char* ws;
};

constexpr int XSLOT = 32768;
constexpr int L_W = 3 * XSLOT;
constexpr int LDS1 = 163840;
constexpr int L_AUX = L_W + 5 * 10240;
constexpr int L_RAW = L_AUX;
constexpr int L_GW = L_RAW + 7168;
constexpr int L_ZERO = L_GW + 2048;
constexpr int L_ONES = L_ZERO + 16;
static_assert(L_ONES + 16 <= LDS1, "lds");
constexpr int IMG_PITCH = 480;

DI int pt_krow(int ks, int slot) {
  const int g = slot >> 3, j = slot & 7;
  if (ks < KS_H) {
    int G = ks / 3, e = ks % 3;
    int t = (e == 0) ? (j >> 2) : (e == 1) ? 3 + (j >> 2) : ((j >> 2) ? 5 : 2);
    return 96 * G + 16 * t + 4 * g + (j & 3);
  }
  const int k = ks - KS_H;
  if (k < 16) return (2 * k + (g >> 1)) * 20 + (g & 1) * 8 + j;
  return (8 * (k - 16) + 2 * g + (j >> 2)) * 20 + 16 + (j & 3);
}
DI void pretile_issue(const P1& p, unsigned ldsbase, int unit, int wave, int lane) {
  const int ks = unit >> 2, q4 = unit & 3;
  const u16* src = (ks < KS_H) ? p.wd : p.wo;
#pragma unroll
  for (int i = 0; i < 2; ++i) {
    const int n = wave + 8 * i;
    const int slot = 2 * n + (lane >= 20), j = lane >= 20 ? lane - 20 : lane;
    const int kr = pt_krow(ks, slot & 31);
    glds16m(src, (unsigned)(kr * D_ + q4 * 160 + (j < 20 ? j : 0) * 8) * 2u, ldsbase + n * 640, (1ull << 40) - 1);
  }
}
DI void pretile_finish(const P1& p, const char* img, int unit, int wave, int lane) {
  const int ks = unit >> 2, q4 = unit & 3;
  const int g = lane >> 4, q = (lane >> 2) & 3, pp = lane & 3;
#pragma unroll
  for (int i = 0; i < 2; ++i) {
    const int n = wave + 8 * i;
    if (n < 10) {
      const char* a = img + (8 * g + q) * 320 + n * 32 + pp * 8;
      s16x8 v = cat(tr_read(a), tr_read(a + 4 * 320));
      const int cb = 2 * q4 + (n >= 5), nt = n >= 5 ? n - 5 : n;
      *(s16x8*)(p.ws + WS_WB + ((size_t)(cb * KS_T + ks) * 5 + nt) * 1024 + lane * 16) = v;
    }
  }
}

constexpr int vm_after(int hn) {
  int n = 0, last = 0;
  const int tneed = hn >> 1;
  const int pk[8] = {0, 1, 1, 0, 1, 1, 0, 1}, pi[8] = {0, 0, 1, 1, 2, 3, 2, 4};
  for (int e = 0; e < 8; ++e) {
    n += pk[e] == 0 ? 4 : 2;
    if ((pk[e] == 0 && pi[e] == tneed) || (pk[e] == 1 && pi[e] == hn)) last = n;
  }
  for (int h = 0; h < hn - 1; ++h) {
    if (h + 5 < 20) { n += 2; if (h + 5 == hn) last = n; }
    const int t = (h & 1) ? (h + 5) / 2 : (h + 4) / 2;
    if (h >= 1 && t < 10) { n += 2; if (t == tneed) last = n; }
  }
  return n - last;
}

template <int TYPE>
DI void k1_tile(const P1& p, char* smem, int rb, int ct) {
  constexpr int NT = (TYPE == 2) ? 6 : 5;
  constexpr int WSLOT = (TYPE == 2) ? 12288 : 10240;
  constexpr int WP = (TYPE == 2) ? 192 : 320;
  constexpr unsigned WPITCH = (TYPE == 2) ? F_ * 2 : NQKV * 2;
  const int tid = threadIdx.x, lane = tid & 63;
  const int wave = __builtin_amdgcn_readfirstlane(tid >> 6);
  const int wm = wave & 3, wn = wave >> 2;
  const int g = lane >> 4, c = lane & 15, q = (lane >> 2) & 3, pp = lane & 3;
  const unsigned lbase = lds_addr(smem);

  const char* xbase = (const char*)p.x + (size_t)rb * 256 * 1280;
  unsigned xoff;
  {
    const int row = wave * 8 + (lane >> 3);
    xoff = row * 1280 + (((lane & 7) ^ ((row >> 1) & 7)) << 4);
  }
  const char* wsrc[2];
  unsigned woff[2];
  if (TYPE == 2) {
    const int r = lane / 12, j0 = lane % 12;
    const int kr = 4 * wave + (r & 3);
    const int j = j0 ^ (((kr >> 3) & 1) << 1);
    wsrc[0] = (const char*)p.wg; wsrc[1] = (const char*)p.wu;
    woff[0] = woff[1] = kr * WPITCH + (ct * 96 + j * 8) * 2;
  } else {
    const int r = lane >= 20, j0 = (lane >= 40) ? 0 : lane - 20 * r;
#pragma unroll
    for (int i = 0; i < 2; ++i) {
      const int kr = 2 * (wave + 8 * i) + r;
      const int j = j0 ^ (((kr >> 3) & 1) << 1);
      int col;
      if (TYPE == 1) col = ct * 160 + j * 8;
      else { const int seg = j / 5, jj = j % 5; col = 640 + (seg & 1) * 640 + ct * 80 + (seg >> 1) * 40 + jj * 8; }
      wsrc[i] = (const char*)p.wqkv;
      woff[i] = kr * WPITCH + col * 2;
    }
  }
  auto issue_x1 = [&](int t, int i) {
    glds16(xbase + (size_t)(i * 81920 + t * 128), xoff, lbase + (t % 3) * XSLOT + wave * 1024 + i * 8192);
  };
  auto issue_w1 = [&](int h, int i) {
    glds16m(wsrc[i] + (size_t)h * 32 * WPITCH, woff[i], lbase + L_W + (h % 5) * WSLOT + wave * (WSLOT / 16) + i * (WSLOT / 2), (1ull << (WSLOT / 256)) - 1);
  };
  auto issue_x = [&](int t) { issue_x1(t, 0); issue_x1(t, 1); issue_x1(t, 2); issue_x1(t, 3); };
  auto issue_w = [&](int h) { issue_w1(h, 0); issue_w1(h, 1); };

  STAMPK(p.ws, 0, 0);
  const bool do_gate = (TYPE == 1) && (ct == 0);
  if (TYPE != 2) {
    constexpr int RAWB = (TYPE == 1) ? 6400 : 3200;
    if (wave < 7) {
      unsigned o = wave * 1024 + lane * 16; if (o > RAWB - 16) o = RAWB - 16;
      glds16((const char*)p.wphi + (size_t)ct * RAWB, o, lbase + L_RAW + wave * 1024);
    } else {
      glds16((const char*)p.gw, lane * 16, lbase + L_GW);
      glds16m((const char*)p.gw, 1024 + (lane & 15) * 16, lbase + L_GW + 1024, 0xffffull);
    }
  }
  issue_x(0); issue_w(0); issue_w(1); issue_x(1); issue_w(2); issue_w(3); issue_x(2); issue_w(4);
  if (TYPE != 2) {
    if (tid < 4) *(unsigned*)(smem + L_ZERO + tid * 4) = 0u;
    if (tid >= 64 && tid < 68) *(unsigned*)(smem + L_ONES + (tid - 64) * 4) = 0x3f803f80u;
  }

  int xrd[2];
  {
    const int f = (c >> 1) & 7;
    xrd[0] = (wm * 64 + c) * 128 + ((g ^ f) << 4);
    xrd[1] = (wm * 64 + c) * 128 + (((4 + g) ^ f) << 4);
  }
  int wrd[NT];
  {
    const int sw = (g & 1) << 5;
#pragma unroll
    for (int t = 0; t < NT; ++t) {
      if (TYPE == 2) {
        const int sub = t / 3, tt = wn * 3 + t % 3;
        wrd[t] = L_W + sub * 6144 + (8 * g + q) * 192 + ((tt * 32 + pp * 8) ^ sw);
      } else {
        wrd[t] = L_W + (8 * g + q) * 320 + ((wn * 160 + pp * 40 + t * 8) ^ sw);
      }
    }
  }

  f32x4 acc[4][NT];
#pragma unroll
  for (int m = 0; m < 4; ++m)
#pragma unroll
    for (int t = 0; t < NT; ++t) acc[m][t] = f32x4{0.f, 0.f, 0.f, 0.f};
  f32x4 gacc[4];
#pragma unroll
  for (int m = 0; m < 4; ++m) gacc[m] = f32x4{0.f, 0.f, 0.f, 0.f};
  const bool gate_wave = do_gate && (wn == 0);

  s16x8 xf[2][4], wf[2][NT];
  auto read_x = [&](int h, int b, int m) {
    xf[b][m] = *(const s16x8*)(smem + ((h >> 1) % 3) * XSLOT + xrd[h & 1] + m * 2048);
  };
  auto read_w = [&](int h, int b, int t) {
    const char* a = smem + wrd[t] + (h % 5) * WSLOT;
    wf[b][t] = cat(tr_read(a), tr_read(a + 4 * WP));
  };

  WAIT_V(vm_after(0)); WAIT_L0(); BAR();
  STAMPK(p.ws, 0, 1); STAMPC(p.ws, 0, 13);
#pragma unroll
  for (int m = 0; m < 4; ++m) read_x(0, 0, m);
#pragma unroll
  for (int t = 0; t < NT; ++t) read_w(0, 0, t);
#pragma unroll
  for (int h = 0; h < 20; ++h) {
    const int b = h & 1;
    if (h + 1 < 20) { WAIT_V(vm_after(h + 1)); }
    WAIT_L0(); BAR();
    if ((h & 3) == 3) STAMPK(p.ws, 0, 2 + (h >> 2));
    const int tx = (h & 1) ? (h + 5) / 2 : (h + 4) / 2, xh = (h & 1) ? 0 : 2;
#pragma unroll
    for (int m = 0; m < 4; ++m) {
      SCHED();
      if (h + 1 < 20) {
        if (m == 0) { read_w(h + 1, b ^ 1, 0); read_x(h + 1, b ^ 1, 0); read_w(h + 1, b ^ 1, 1); }
        if (m == 1) { read_w(h + 1, b ^ 1, 2); read_w(h + 1, b ^ 1, 3); read_x(h + 1, b ^ 1, 1); }
        if (m == 2) { read_w(h + 1, b ^ 1, 4); if (NT == 6) read_w(h + 1, b ^ 1, NT - 1); read_x(h + 1, b ^ 1, 2); read_x(h + 1, b ^ 1, 3); }
      }
#pragma unroll
      for (int t = 0; t < NT; ++t) acc[m][t] = mfma16(wf[b][t], xf[b][m], acc[m][t]);
      if (h + 1 < 20 && m < 3) {
#pragma unroll
        for (int i = 0; i < NT; ++i) {
          __builtin_amdgcn_sched_group_barrier(0x008, 1, 0);
          __builtin_amdgcn_sched_group_barrier(0x100, 1, 0);
        }
      }
      SCHED();
      if (m < 2) { if (h + 5 < 20) issue_w1(h + 5, m); }
      else { if (h >= 1 && tx < 10) issue_x1(tx, xh + (m - 2)); }
    }
    if (TYPE == 1 && gate_wave) {
      s16x8 gf = *(const s16x8*)(smem + L_GW + h * 64 + g * 16);
#pragma unroll
      for (int m = 0; m < 4; ++m) gacc[m] = mfma16(gf, xf[b][m], gacc[m]);
    }
  }
  STAMPK(p.ws, 0, 11); STAMPC(p.ws, 0, 14);

  const int srow0 = rb * 256 + wm * 64;
  if (TYPE == 2) {
    constexpr float C0 = -2.0f * 0.7978845608028654f * 1.4426950408889634f, C1 = C0 * 0.044715f;
#pragma unroll
    for (int m = 0; m < 4; ++m) {
      unsigned hp[3][2];
#pragma unroll
      for (int t = 0; t < 3; ++t) {
        float h[4];
#pragma unroll
        for (int r = 0; r < 4; ++r) {
          const float xg = acc[m][t][r], up = acc[m][t + 3][r];
          const float e = fexp2(xg * __builtin_fmaf(xg * xg, C1, C0));
          h[r] = xg * frcp(1.0f + e) * up;
        }
        hp[t][0] = pack2(h[0], h[1]); hp[t][1] = pack2(h[2], h[3]);
      }
      const int s = srow0 + m * 16;
      char* hb = p.ws + WS_HA + ((size_t)(s >> 6) * KS_T * 4 + ((s >> 4) & 3)) * 1024 + lane * 16;
      *(u32x4*)(hb + (size_t)(ct * 3 + wn) * 4096) = u32x4{hp[0][0], hp[0][1], hp[1][0], hp[1][1]};
      *(u32x2*)(hb + (size_t)(ct * 3 + 2) * 4096 + wn * 8) = u32x2{hp[2][0], hp[2][1]};
    }
    return;
  }

  constexpr float L2E = 1.4426950408889634f;
  s16x8 af[5][3];
  {
    const int src = (TYPE == 1) ? pp : (pp & 1);
    const int hl = (TYPE == 1) ? wn * 4 + pp : wn * 2 + (pp & 1);
    const bool on = (g == src);
    const int a0 = on ? L_RAW + hl * 800 + q * 40 : L_ZERO;
    const int st = on ? 8 : 0, sd = on ? 160 : 0;
    const s16x4 z4 = {0, 0, 0, 0};
    const unsigned sg = (TYPE == 0 && c >= 8) ? 0x80008000u : 0u;
#pragma unroll
    for (int t = 0; t < 5; ++t) {
      af[t][0] = cat(tr_read(smem + a0 + t * st), tr_read(smem + a0 + t * st + sd));
      af[t][1] = cat(tr_read(smem + a0 + t * st + 2 * sd), tr_read(smem + a0 + t * st + 3 * sd));
      af[t][2] = cat(tr_read(smem + a0 + t * st + 4 * sd), z4);
      if (TYPE == 0) {
#pragma unroll
        for (int ks = 0; ks < 3; ++ks) af[t][ks] = __builtin_bit_cast(s16x8, __builtin_bit_cast(u32x4, af[t][ks]) ^ u32x4{sg, sg, sg, sg});
      }
    }
  }
#pragma unroll
  for (int m = 0; m < 4; ++m) {
    s16x8 bq[3];
    {
      u32x4 b0 = {pack2(acc[m][0][0], acc[m][0][1]), pack2(acc[m][0][2], acc[m][0][3]), pack2(acc[m][1][0], acc[m][1][1]), pack2(acc[m][1][2], acc[m][1][3])};
      u32x4 b1 = {pack2(acc[m][2][0], acc[m][2][1]), pack2(acc[m][2][2], acc[m][2][3]), pack2(acc[m][3][0], acc[m][3][1]), pack2(acc[m][3][2], acc[m][3][3])};
      u32x4 b2 = {pack2(acc[m][4][0], acc[m][4][1]), pack2(acc[m][4][2], acc[m][4][3]), 0u, 0u};
      bq[0] = __builtin_bit_cast(s16x8, b0); bq[1] = __builtin_bit_cast(s16x8, b1); bq[2] = __builtin_bit_cast(s16x8, b2);
    }
    f32x4 pr[5];
#pragma unroll
    for (int t = 0; t < 5; ++t) pr[t] = f32x4{0.f, 0.f, 0.f, 0.f};
#pragma unroll
    for (int ks = 0; ks < 3; ++ks)
#pragma unroll
      for (int t = 0; t < 5; ++t) pr[t] = mfma16(af[t][ks], bq[ks], pr[t]);
    const int s = srow0 + m * 16 + c;
    if (TYPE == 1) {
      const int head = ct * 8 + wn * 4 + g;
      u32x4* dst = (u32x4*)(p.ws + WS_PQ + ((size_t)head * S_ + s) * 48);
      dst[0] = u32x4{pack2(pr[0][0], pr[0][1]), pack2(pr[0][2], pr[0][3]), pack2(pr[1][0], pr[1][1]), pack2(pr[1][2], pr[1][3])};
      dst[1] = u32x4{pack2(pr[2][0], pr[2][1]), 0u, pack2(pr[2][2], pr[2][3]), pack2(pr[3][0], pr[3][1])};
      dst[2] = u32x4{pack2(pr[3][2], pr[3][3]), pack2(pr[4][0], pr[4][1]), pack2(pr[4][2], pr[4][3]), 0u};
      if (gate_wave && g == 0) {
        const float lg = gacc[m][0];
        ((float*)(p.ws + WS_G))[s] = frcp(1.0f + fexp2(-lg * L2E));
      }
    } else {
      float mx = pr[0][0];
#pragma unroll
      for (int t = 0; t < 5; ++t)
#pragma unroll
        for (int r = 0; r < 4; ++r) mx = fmaxf(mx, pr[t][r]);
      const float mxl = -mx * L2E;
      f32x2 e1[10], s2 = {0.f, 0.f};
#pragma unroll
      for (int t = 0; t < 5; ++t)
#pragma unroll
        for (int r = 0; r < 2; ++r) {
          const f32x2 a = __builtin_elementwise_fma(f32x2{pr[t][2 * r], pr[t][2 * r + 1]}, f32x2{L2E, L2E}, f32x2{mxl, mxl});
          const f32x2 e = {fexp2(a[0]), fexp2(a[1])};
          e1[t * 2 + r] = e; s2 += e;
        }
      const float i1 = frcp(s2[0] + s2[1]);
      unsigned pk1[10];
#pragma unroll
      for (int j = 0; j < 10; ++j) { const f32x2 v = e1[j] * f32x2{i1, i1}; pk1[j] = pack2(v[0], v[1]); }
      char* rowp = smem + (wm * 64 + m * 16 + c) * IMG_PITCH;
      char* pd = rowp + (wn * 2 + (g & 1)) * 80 + (g >> 1) * 40;
#pragma unroll
      for (int j = 0; j < 5; ++j) *(u32x2*)(pd + j * 8) = u32x2{pk1[2 * j], pk1[2 * j + 1]};
      if (g >= 2) {
        char* vd = rowp + 320 + (wn * 2 + (g - 2)) * 40;
        const u32x4 b0 = __builtin_bit_cast(u32x4, bq[0]), b1 = __builtin_bit_cast(u32x4, bq[1]), b2 = __builtin_bit_cast(u32x4, bq[2]);
        *(u32x2*)(vd + 0) = u32x2{b0[0], b0[1]};
        *(u32x2*)(vd + 8) = u32x2{b0[2], b0[3]};
        *(u32x2*)(vd + 16) = u32x2{b1[0], b1[1]};
        *(u32x2*)(vd + 24) = u32x2{b1[2], b1[3]};
        *(u32x2*)(vd + 32) = u32x2{b2[0], b2[1]};
      }
    }
  }
  if (TYPE == 0) {
    WAIT_L0(); BAR();
    STAMPK(p.ws, 0, 10);
    const int hh = wave & 3, u = wave >> 2;
    const int abase = (8 * g + q) * IMG_PITCH + hh * 80;
    const int a0 = abase + pp * 8, a1 = abase + 32 + pp * 8, a2 = abase + 64 + (pp & 1) * 8;
    int b0, bstep;
    if (u == 0) { b0 = (8 * g + q) * IMG_PITCH + 320 + hh * 40 + pp * 8; bstep = IMG_PITCH; }
    else if (pp != 1) { b0 = (8 * g + q) * IMG_PITCH + 320 + hh * 40 + 32; bstep = IMG_PITCH; }
    else { b0 = L_ONES; bstep = 0; }
    f32x4 kv[3];
#pragma unroll
    for (int a = 0; a < 3; ++a) kv[a] = f32x4{0.f, 0.f, 0.f, 0.f};
#pragma unroll
    for (int ks = 0; ks < 8; ++ks) {
      const int ro = ks * 32 * IMG_PITCH;
      s16x8 bf = cat(tr_read(smem + b0 + ks * 32 * bstep), tr_read(smem + b0 + (ks * 32 + 4) * bstep));
      s16x8 f0 = cat(tr_read(smem + a0 + ro), tr_read(smem + a0 + ro + 4 * IMG_PITCH));
      s16x8 f1 = cat(tr_read(smem + a1 + ro), tr_read(smem + a1 + ro + 4 * IMG_PITCH));
      s16x8 f2 = cat(tr_read(smem + a2 + ro), tr_read(smem + a2 + ro + 4 * IMG_PITCH));
      kv[0] = mfma16(f0, bf, kv[0]);
      kv[1] = mfma16(f1, bf, kv[1]);
      kv[2] = mfma16(f2, bf, kv[2]);
    }
    const int head = ct * 4 + hh;
    const int d = 16 * u + c;
    if (d <= 20) {
      float* sl = (float*)(p.ws + WS_SLAB) + ((size_t)(rb * 32 + head) * 21 + d) * 40 + 4 * g;
      *(f32x4*)(sl) = kv[0];
      *(f32x4*)(sl + 16) = kv[1];
      if (g < 2) *(f32x4*)(sl + 32) = kv[2];
    }
  }
}

__global__ __launch_bounds__(512, 2) void k1(P1 p) {
  __shared__ __attribute__((aligned(1024))) char smem[LDS1];
  const int bid = blockIdx.x;
  const int xcd = bid & 7, idx = bid >> 3;
  const int rb = (xcd >> 2) * 4 + (idx & 3);
  const int slot = (idx >> 2) * 4 + (xcd & 3);
  if (slot < 30) {
    if (slot < 8) k1_tile<0>(p, smem, rb, slot);
    else if (slot < 12) k1_tile<1>(p, smem, rb, slot - 8);
    else k1_tile<2>(p, smem, rb, slot - 12);
    STAMPK(p.ws, 0, 12);
  } else {
    const int lane = threadIdx.x & 63, wave = __builtin_amdgcn_readfirstlane(threadIdx.x >> 6);
    const int sp = rb * 2 + (slot - 30);
    STAMPK(p.ws, 0, 0);
    const int u0 = sp * 19, u1 = (u0 + 19 < 296) ? u0 + 19 : 296;
    for (int u = u0; u < u0 + 10; ++u) pretile_issue(p, lds_addr(smem) + (u - u0) * 10240, u, wave, lane);
    for (int u = u0 + 10; u < u0 + 15 && u < u1; ++u) pretile_issue(p, lds_addr(smem) + (u - u0) * 10240, u, wave, lane);
    for (int r = 0; r < 2; ++r) {
      char* dst = p.ws + WS_HA + ((size_t)(2 * sp + r) * KS_T + KS_H) * 4096;
      for (int e = threadIdx.x; e < KS_O * 4096 / 16; e += 512) *(u32x4*)(dst + e * 16) = u32x4{~0u, ~0u, ~0u, ~0u};
    }
    WAIT_V(0); BAR();
    for (int u = u0; u < u0 + 4; ++u) pretile_finish(p, smem + (u - u0) * 10240, u, wave, lane);
    WAIT_L0(); BAR();
    for (int u = u0 + 15; u < u1; ++u) pretile_issue(p, lds_addr(smem) + (u - u0 - 15) * 10240, u, wave, lane);
    for (int u = u0 + 4; u < u0 + 15 && u < u1; ++u) pretile_finish(p, smem + (u - u0) * 10240, u, wave, lane);
    WAIT_V(0); BAR();
    for (int u = u0 + 15; u < u1; ++u) pretile_finish(p, smem + (u - u0 - 15) * 10240, u, wave, lane);
    STAMPK(p.ws, 0, 12);
  }
}

struct P2 { char* ws; u16* out; };
constexpr int L2_RED = 0;
constexpr int L2_RED2 = 61440;
constexpr int L2_KV = L2_RED2 + 81920;
constexpr int L2_FLAG = L2_KV + 32 * 144;
constexpr int LDS2 = L2_FLAG + 64;

__global__ __launch_bounds__(512, 2) void k2(P2 p) {
  __shared__ __attribute__((aligned(1024))) char smem[LDS2];
  const int bid = blockIdx.x;
  const int xcd = bid & 7, idx = bid >> 3;
  const int rb64 = xcd * 4 + (idx >> 3), cb = idx & 7;
  const int tid = threadIdx.x, lane = tid & 63;
  const int wave = __builtin_amdgcn_readfirstlane(tid >> 6);
  const int g = lane >> 4, c = lane & 15;
  volatile LAS unsigned* flags = (volatile LAS unsigned*)(smem + L2_FLAG);
  const auto rsA = __builtin_amdgcn_make_buffer_rsrc((void*)(p.ws + WS_HA + (size_t)rb64 * KS_T * 4096), 0, KS_T * 4096, 0x00020000);
  const auto rsB = __builtin_amdgcn_make_buffer_rsrc((void*)(p.ws + WS_WB + (size_t)cb * KS_T * 5120), 0, KS_T * 5120, 0x00020000);
  const int l16 = lane * 16;
  constexpr int DEPTH = 4;
  s16x8 af[DEPTH][4], bf[DEPTH][5];
  f32x4 acc[4][5];
#pragma unroll
  for (int m = 0; m < 4; ++m)
#pragma unroll
    for (int n = 0; n < 5; ++n) acc[m][n] = f32x4{0.f, 0.f, 0.f, 0.f};
  auto load_b = [&](int ks, int slot) {
#pragma unroll
    for (int n = 0; n < 5; ++n) bf[slot][n] = __builtin_bit_cast(s16x8, __builtin_amdgcn_raw_buffer_load_b128(rsB, l16 + (n & 3) * 1024, ks * 5120 + (n >> 2) * 4096, 0));
  };
  auto mma = [&](int slot) {
#pragma unroll
    for (int m = 0; m < 4; ++m)
#pragma unroll
      for (int n = 0; n < 5; ++n) acc[m][n] = mfma16(bf[slot][n], af[slot][m], acc[m][n]);
  };

  if (wave < 4) {
    auto load_h = [&](int i, int slot) {
      int ks = wave + 4 * i; if (ks > KS_H - 1) ks = KS_H - 1;
#pragma unroll
      for (int m = 0; m < 4; ++m) af[slot][m] = __builtin_bit_cast(s16x8, __builtin_amdgcn_raw_buffer_load_b128(rsA, l16 + m * 1024, ks * 4096, 0));
      load_b(ks, slot);
    };
#pragma unroll
    for (int i = 0; i < DEPTH; ++i) load_h(i, i);
    SCHED();
    BAR();
    STAMPK(p.ws, 1, 0);
    const int nh = (wave < 2) ? 14 : 13;
    float gm[4];
#pragma unroll
    for (int i = 0; i < 14; ++i) {
      if (i < 13 || nh == 14) mma(i % DEPTH);
      SCHED();
      if (i + DEPTH < 14) load_h(i + DEPTH, i % DEPTH);
      if (i == 14 - DEPTH) {
#pragma unroll
        for (int m = 0; m < 4; ++m) gm[m] = ((const float*)(p.ws + WS_G))[rb64 * 64 + m * 16 + c];
      }
      SCHED();
    }
    STAMPK(p.ws, 1, 4);
    auto tail = [&](auto WC) {
      constexpr int W = decltype(WC)::value;
#pragma unroll
      for (int m = 0; m < 4; ++m) {
        const float sc = 1.0f - gm[m];
#pragma unroll
        for (int n = 0; n < 5; ++n) acc[m][n] *= sc;
        if (m != W) {
          constexpr int dummy = 0; (void)dummy;
          const int sl = (W - m - 1) & 3;
#pragma unroll
          for (int n = 0; n < 5; ++n) *(f32x4*)(smem + L2_RED + ((m * 3 + sl) * 5 + n) * 1024 + l16) = acc[m][n];
        }
      }
      WAIT_L0(); BAR();
      STAMPK(p.ws, 1, 6);
      f32x4 r[5];
#pragma unroll
      for (int n = 0; n < 5; ++n) r[n] = acc[W][n];
#pragma unroll
      for (int sl = 0; sl < 3; ++sl)
#pragma unroll
        for (int n = 0; n < 5; ++n) r[n] += *(const f32x4*)(smem + L2_RED + ((W * 3 + sl) * 5 + n) * 1024 + l16);
#pragma unroll
      for (int hw = 0; hw < 4; ++hw)
#pragma unroll
        for (int n = 0; n < 5; ++n) r[n] += *(const f32x4*)(smem + L2_RED2 + ((hw * 4 + W) * 5 + n) * 1024 + l16);
      u16* o = p.out + (size_t)(rb64 * 64 + W * 16 + c) * D_ + cb * 80 + 4 * g;
#pragma unroll
      for (int n = 0; n < 5; ++n) *(u32x2*)(o + n * 16) = u32x2{pack2(r[n][0], r[n][1]), pack2(r[n][2], r[n][3])};
    };
    if (wave == 0) tail(std::integral_constant<int, 0>{});
    else if (wave == 1) tail(std::integral_constant<int, 1>{});
    else if (wave == 2) tail(std::integral_constant<int, 2>{});
    else tail(std::integral_constant<int, 3>{});
    STAMPK(p.ws, 1, 7);
    return;
  }

  {
    constexpr float L2E = 1.4426950408889634f;
    const int hw = wave - 4, ht = tid - 256, head = idx;
    f32x4 sl[8];
    {
      const int e = ht < 210 ? ht : 209;
#pragma unroll
      for (int r = 0; r < 8; ++r) sl[r] = *(const f32x4*)(p.ws + WS_SLAB + (size_t)(r * 32 + head) * 3360 + e * 16);
    }
    u32x2 pw[4][3];
    float gv[4];
#pragma unroll
    for (int i = 0; i < 4; ++i) {
      const int s = xcd * 256 + (hw * 4 + i) * 16 + c;
      const char* src = p.ws + WS_PQ + ((size_t)head * S_ + s) * 48 + (g & 1) * 24;
#pragma unroll
      for (int j = 0; j < 3; ++j) pw[i][j] = *(const u32x2*)(src + j * 8);
      gv[i] = ((const float*)(p.ws + WS_G))[s];
    }
    SCHED();
    if (ht < 4) flags[ht] = 0u;
    for (int e = ht; e < 32 * 144 / 16; e += 256) *(u32x4*)(smem + L2_KV + e * 16) = u32x4{0u, 0u, 0u, 0u};
    WAIT_L0(); BAR();
    STAMPK(p.ws, 1, 0);
    if (ht < 210) {
      const f32x4 a = ((sl[0] + sl[1]) + (sl[2] + sl[3])) + ((sl[4] + sl[5]) + (sl[6] + sl[7]));
      const int d = ht / 10, f = (ht % 10) * 4;
      *(unsigned*)(smem + L2_KV + d * 144 + (16 * (f / 10) + f % 10) * 2) = pack2(a[0], a[1]);
      *(unsigned*)(smem + L2_KV + d * 144 + (16 * ((f + 2) / 10) + (f + 2) % 10) * 2) = pack2(a[2], a[3]);
    }
    WAIT_L0();
    if (lane == 0) __hip_atomic_fetch_add((LAS unsigned*)&flags[0], 1u, __ATOMIC_RELAXED, __HIP_MEMORY_SCOPE_WORKGROUP);
    s16x8 pf[4][2];
    const unsigned sg = (g >= 2) ? 0x80008000u : 0u;
#pragma unroll
    for (int i = 0; i < 4; ++i) {
      const unsigned w[5] = {pw[i][0][0] ^ sg, pw[i][0][1] ^ sg, pw[i][1][0] ^ sg, pw[i][1][1] ^ sg, pw[i][2][0] ^ sg};
      f32x2 v[5];
#pragma unroll
      for (int j = 0; j < 5; ++j) v[j] = f32x2{__uint_as_float(w[j] << 16), __uint_as_float(w[j] & 0xffff0000u)};
      float mx = v[0][0];
#pragma unroll
      for (int j = 0; j < 5; ++j) mx = fmaxf(fmaxf(mx, v[j][0]), v[j][1]);
      { const auto r = __builtin_amdgcn_permlane16_swap(__float_as_uint(mx), __float_as_uint(mx), false, false);
        const unsigned r0 = r[0], r1 = r[1]; mx = fmaxf(__uint_as_float(r0), __uint_as_float(r1)); }
      const float mxl = -mx * L2E;
      f32x2 s2 = {0.f, 0.f};
#pragma unroll
      for (int j = 0; j < 5; ++j) {
        const f32x2 a = __builtin_elementwise_fma(v[j], f32x2{L2E, L2E}, f32x2{mxl, mxl});
        v[j] = f32x2{fexp2(a[0]), fexp2(a[1])}; s2 += v[j];
      }
      float sm = s2[0] + s2[1];
      { const auto r = __builtin_amdgcn_permlane16_swap(__float_as_uint(sm), __float_as_uint(sm), false, false);
        const unsigned r0 = r[0], r1 = r[1]; sm = __uint_as_float(r0) + __uint_as_float(r1); }
      const float inv = frcp(sm);
      unsigned pk[5];
#pragma unroll
      for (int j = 0; j < 5; ++j) { const f32x2 t = v[j] * f32x2{inv, inv}; pk[j] = pack2(t[0], t[1]); }
      pf[i][0] = __builtin_bit_cast(s16x8, u32x4{pk[0], pk[1], pk[2], pk[3]});
      pf[i][1] = __builtin_bit_cast(s16x8, u32x4{pk[4], 0u, 0u, 0u});
    }
    while (flags[0] < 4u) __builtin_amdgcn_s_sleep(1);
    asm volatile("" ::: "memory");
    STAMPK(p.ws, 1, 1);
    s16x8 kf[3][2];
    {
      const int gp = c >> 2, r = c & 3;
      const int d0 = (gp < 3 ? gp : 2) * 8 + r, d1 = (gp & 1) * 8 + 4 + r;
#pragma unroll
      for (int ks = 0; ks < 2; ++ks) {
        kf[0][ks] = *(const s16x8*)(smem + L2_KV + d0 * 144 + g * 32 + ks * 16);
        kf[1][ks] = *(const s16x8*)(smem + L2_KV + d1 * 144 + g * 32 + ks * 16);
        kf[2][ks] = *(const s16x8*)(smem + L2_KV + 20 * 144 + g * 32 + ks * 16);
      }
    }
    const auto rsO = __builtin_amdgcn_make_buffer_rsrc((void*)(p.ws + WS_HA + (size_t)xcd * 4 * KS_T * 4096), 0, 4 * KS_T * 4096, 0x00020000);
#pragma unroll
    for (int i = 0; i < 4; ++i) {
      f32x4 n0 = {0.f, 0.f, 0.f, 0.f}, n1 = n0, dn = n0;
#pragma unroll
      for (int ks = 0; ks < 2; ++ks) {
        n0 = mfma16(kf[0][ks], pf[i][ks], n0);
        n1 = mfma16(kf[1][ks], pf[i][ks], n1);
        dn = mfma16(kf[2][ks], pf[i][ks], dn);
      }
      const float sc = gv[i] / (dn[0] + 1e-6f);
      const int fb = (hw * KS_T * 4 + i) * 1024;
      u32x4 v = {pack2(n0[0] * sc, n0[1] * sc), pack2(n0[2] * sc, n0[3] * sc), pack2(n1[0] * sc, n1[1] * sc), pack2(n1[2] * sc, n1[3] * sc)};
#pragma unroll
      for (int j = 0; j < 4; ++j) if (v[j] == ~0u) v[j] = 0x7fc07fc0u;
      if (g < 2) {
        __builtin_amdgcn_raw_buffer_store_b128(v, rsO, fb + (KS_H + (head >> 1)) * 4096 + ((2 * (head & 1) + g) * 16 + c) * 16, 0, 16);
      } else if (g == 2) {
        __builtin_amdgcn_raw_buffer_store_b64(u32x2{v[0], v[1]}, rsO, fb + (KS_H + 16 + (head >> 3)) * 4096 + (head & 1) * 512 + (((head & 7) >> 1) * 16 + c) * 8, 0, 16);
      }
    }
    STAMPK(p.ws, 1, 2);
#pragma unroll
    for (int i = 0; i < 4; ++i) load_b(KS_H + hw + 4 * i, i);
    SCHED();
    for (unsigned spins = 0; spins < (1u << 20); ++spins) {
      unsigned allw = ~0u * 0u;
#pragma unroll
      for (int i = 0; i < 4; ++i)
#pragma unroll
        for (int m = 0; m < 4; ++m)
          af[i][m] = __builtin_bit_cast(s16x8, __builtin_amdgcn_raw_buffer_load_b128(rsA, l16 + m * 1024, (KS_H + hw + 4 * i) * 4096, 16));
#pragma unroll
      for (int i = 0; i < 4; ++i)
#pragma unroll
        for (int m = 0; m < 4; ++m) {
          const u32x4 w = __builtin_bit_cast(u32x4, af[i][m]);
          allw |= (w[0] == ~0u) | (w[1] == ~0u) | (w[2] == ~0u) | (w[3] == ~0u);
        }
      if (!__any(allw)) break;
      __builtin_amdgcn_s_sleep(4);
    }
    STAMPK(p.ws, 1, 3);
    mma(0);
    SCHED();
    load_b(KS_H + 16 + hw, 0);
    for (unsigned spins = 0; spins < (1u << 20); ++spins) {
      unsigned allw = 0u;
#pragma unroll
      for (int m = 0; m < 4; ++m) {
        const u32x2 lo = __builtin_amdgcn_raw_buffer_load_b64(rsA, lane * 8 + m * 1024, (KS_H + 16 + hw) * 4096, 16);
        const u32x2 hi = __builtin_amdgcn_raw_buffer_load_b64(rsA, lane * 8 + 512 + m * 1024, (KS_H + 16 + hw) * 4096, 16);
        af[0][m] = __builtin_bit_cast(s16x8, u32x4{lo[0], lo[1], hi[0], hi[1]});
        allw |= (lo[0] == ~0u) | (lo[1] == ~0u) | (hi[0] == ~0u) | (hi[1] == ~0u);
      }
      if (!__any(allw)) break;
      __builtin_amdgcn_s_sleep(4);
    }
    mma(1); mma(2); mma(3);
    mma(0);
#pragma unroll
    for (int m = 0; m < 4; ++m)
#pragma unroll
      for (int n = 0; n < 5; ++n) *(f32x4*)(smem + L2_RED2 + ((hw * 4 + m) * 5 + n) * 1024 + l16) = acc[m][n];
    STAMPK(p.ws, 1, 5);
    WAIT_L0(); BAR();
  }
}

extern "C" void kernel_launch(void* const* d_in, const int* in_sizes, int n_in,
                              void* d_out, int out_size, void* d_ws, size_t ws_size,
                              hipStream_t stream) {
  P1 p1;
  p1.x = (const u16*)d_in[0]; p1.wqkv = (const u16*)d_in[1]; p1.wphi = (const u16*)d_in[2]; p1.wo = (const u16*)d_in[3];
  p1.wg = (const u16*)d_in[5]; p1.wu = (const u16*)d_in[6]; p1.wd = (const u16*)d_in[7]; p1.gw = (const u16*)d_in[8];
  p1.ws = (char*)d_ws;
  k1<<<256, 512, 0, stream>>>(p1);
  P2 p2; p2.ws = (char*)d_ws; p2.out = (u16*)d_out;
  k2<<<256, 512, 0, stream>>>(p2);
}
```

```cpp
#include <hip/hip_runtime.h>
#include <hip/hip_bf16.h>
#include <stdint.h>
#include <type_traits>
#include <utility>
#define STAMPK(ws, kern, i) do {} while (0)
#define STAMPC(ws, kern, i) do {} while (0)
#define STAMP_DECL
#define STAMP_DUMP(ws, kern) do {} while (0)
#define STAMP_ARG
#define STAMP_PASS

typedef __attribute__((ext_vector_type(8))) short s16x8;
typedef __attribute__((ext_vector_type(4))) short s16x4;
typedef __attribute__((ext_vector_type(8))) __bf16 bf16x8;
typedef __attribute__((ext_vector_type(2))) __bf16 bf16x2;
typedef __attribute__((ext_vector_type(4))) float f32x4;
typedef __attribute__((ext_vector_type(2))) float f32x2;
typedef __attribute__((ext_vector_type(4))) unsigned u32x4;
typedef __attribute__((ext_vector_type(2))) unsigned u32x2;
typedef unsigned short u16;
#define GAS __attribute__((address_space(1)))
#define LAS __attribute__((address_space(3)))
#define DI __device__ __forceinline__

constexpr int S_ = 2048, D_ = 640, F_ = 1728, NQKV = 1920;
constexpr int KS_H = 54, KS_O = 20, KS_T = 74;

constexpr size_t WS_HA   = 0;
constexpr size_t WS_WB   = WS_HA + (size_t)32 * KS_T * 4096;
constexpr size_t WS_PQ = WS_WB + (size_t)8 * KS_T * 5120;
constexpr size_t WS_SLAB = WS_PQ + (size_t)32 * 2048 * 48;
constexpr size_t WS_G    = WS_SLAB + (size_t)8 * 32 * 3360;
constexpr size_t WS_CNT  = WS_G + 2048 * 4;

DI f32x4 mfma16(s16x8 a, s16x8 b, f32x4 c) {
  return __builtin_amdgcn_mfma_f32_16x16x32_bf16(__builtin_bit_cast(bf16x8, a), __builtin_bit_cast(bf16x8, b), c, 0, 0, 0);
}
DI unsigned pack2(float a, float b) {
  bf16x2 v = {(__bf16)a, (__bf16)b};
  return __builtin_bit_cast(unsigned, v);
}
DI s16x4 tr_read(const char* lds) {
  return __builtin_amdgcn_ds_read_tr16_b64_v4i16((LAS s16x4*)(lds));
}
DI s16x8 cat(s16x4 a, s16x4 b) { return __builtin_shufflevector(a, b, 0, 1, 2, 3, 4, 5, 6, 7); }
DI void glds16(const void* sbase, unsigned voff, unsigned ldsaddr) {
  asm volatile("s_mov_b32 m0, %2\n\ts_nop 0\n\tglobal_load_lds_dwordx4 %0, %1" :: "v"(voff), "s"(sbase), "s"(ldsaddr) : "memory");
}
DI void glds16m(const void* sbase, unsigned voff, unsigned ldsaddr, unsigned long long mask) {
  asm volatile("s_mov_b64 exec, %3\n\ts_mov_b32 m0, %2\n\ts_nop 0\n\tglobal_load_lds_dwordx4 %0, %1\n\ts_mov_b64 exec, -1"
               :: "v"(voff), "s"(sbase), "s"(ldsaddr), "s"(mask) : "memory");
}
DI void touch4(const void* sbase, unsigned voff, unsigned ldsaddr) {
  asm volatile("s_mov_b32 m0, %2\n\ts_nop 0\n\tglobal_load_lds_dword %0, %1" :: "v"(voff), "s"(sbase), "s"(ldsaddr) : "memory");
}
DI unsigned lds_addr(const char* p) { return (unsigned)(size_t)(const LAS char*)p; }
DI float fexp2(float x) { return __builtin_amdgcn_exp2f(x); }
DI float frcp(float x) { return __builtin_amdgcn_rcpf(x); }
#define WAIT_V(n) asm volatile("s_waitcnt vmcnt(%0)" :: "n"(n) : "memory")
#define WAIT_L0() asm volatile("s_waitcnt lgkmcnt(0)" ::: "memory")
#define BAR() __builtin_amdgcn_s_barrier()
#define SCHED() __builtin_amdgcn_sched_barrier(0)

template <class F, int... I> DI void static_for_impl(F&& f, std::integer_sequence<int, I...>) { (f(std::integral_constant<int, I>{}), ...); }
template <int N, class F> DI void static_for(F&& f) { static_for_impl(f, std::make_integer_sequence<int, N>{}); }

struct P1 {
  const u16* x; const u16* wqkv; const u16* wphi; const u16* wo;
  const u16* wg; const u16* wu; const u16* wd; const u16* gw;
  char* ws;
};

constexpr int XSLOT = 32768;
constexpr int L_W = 3 * XSLOT;
constexpr int LDS1 = 163840;
constexpr int L_AUX = L_W + 5 * 10240;
constexpr int L_RAW = L_AUX;
constexpr int L_GW = L_RAW + 7168;
constexpr int L_ZERO = L_GW + 2048;
constexpr int L_ONES = L_ZERO + 16;
static_assert(L_ONES + 16 <= LDS1, "lds");
constexpr int IMG_PITCH = 480;

DI int pt_krow(int ks, int slot) {
  const int g = slot >> 3, j = slot & 7;
  if (ks < KS_H) {
    int G = ks / 3, e = ks % 3;
    int t = (e == 0) ? (j >> 2) : (e == 1) ? 3 + (j >> 2) : ((j >> 2) ? 5 : 2);
    return 96 * G + 16 * t + 4 * g + (j & 3);
  }
  const int k = ks - KS_H;
  if (k < 16) return (2 * k + (g >> 1)) * 20 + (g & 1) * 8 + j;
  return (8 * (k - 16) + 2 * g + (j >> 2)) * 20 + 16 + (j & 3);
}
DI void pretile_issue(const P1& p, unsigned ldsbase, int unit, int wave, int lane) {
  const int ks = unit >> 2, q4 = unit & 3;
  const u16* src = (ks < KS_H) ? p.wd : p.wo;
#pragma unroll
  for (int i = 0; i < 2; ++i) {
    const int n = wave + 8 * i;
    const int slot = 2 * n + (lane >= 20), j = lane >= 20 ? lane - 20 : lane;
    const int kr = pt_krow(ks, slot & 31);
    glds16m(src, (unsigned)(kr * D_ + q4 * 160 + (j < 20 ? j : 0) * 8) * 2u, ldsbase + n * 640, (1ull << 40) - 1);
  }
}
DI void pretile_finish(const P1& p, const char* img, int unit, int wave, int lane) {
  const int ks = unit >> 2, q4 = unit & 3;
  const int g = lane >> 4, q = (lane >> 2) & 3, pp = lane & 3;
#pragma unroll
  for (int i = 0; i < 2; ++i) {
    const int n = wave + 8 * i;
    if (n < 10) {
      const char* a = img + (8 * g + q) * 320 + n * 32 + pp * 8;
      s16x8 v = cat(tr_read(a), tr_read(a + 4 * 320));
      const int cb = 2 * q4 + (n >= 5), nt = n >= 5 ? n - 5 : n;
      *(s16x8*)(p.ws + WS_WB + ((size_t)(cb * KS_T + ks) * 5 + nt) * 1024 + lane * 16) = v;
    }
  }
}

#define K1_CODE_BYTES 57344u
#define K2_CODE_BYTES 14336u
#define TP3 1
#define TP4 3
#define LT2 1
#define SPDELAY 1
constexpr int OPX(int t, int i) { return 0x100 | (t << 2) | i; }
constexpr int OPW(int h, int i) { return 0x200 | (h << 1) | i; }
constexpr int sched_op(int ph, int k) {
  if (ph == -2) { const int a[9] = {OPX(0, 0), OPX(0, 1), OPX(0, 2), OPX(0, 3), OPW(0, 0), OPW(0, 1), OPW(1, 0), OPW(1, 1), 0x300}; return k < 9 ? a[k] : 0; }
  if (ph == LT2 && k == 8) return 0x301;
  if (ph == -1) { const int a[6] = {OPX(1, 0), OPX(1, 1), OPX(1, 2), OPX(1, 3), OPW(2, 0), OPW(2, 1)}; return k < 6 ? a[k] : 0; }
  if (ph == 0) { const int a[8] = {OPW(3, 0), OPW(3, 1), OPX(2, 0), OPX(2, 1), OPX(2, 2), OPX(2, 3), OPW(4, 0), OPW(4, 1)}; return k < 8 ? a[k] : 0; }
  if (ph == 1) { const int a[8] = {OPW(5, 0), OPW(5, 1), OPW(6, 0), OPW(6, 1), OPX(3, 0), 0, OPX(3, 1), 0}; return k < 8 ? a[k] : 0; }
  if (k >= 8 || (k & 1)) return 0;
  if (k < 4) return (ph + 5 < 20) ? OPW(ph + 5, k >> 1) : 0;
  const int t = (ph & 1) ? (ph + 5) / 2 : (ph + 4) / 2, xh = (ph & 1) ? 0 : 2;
  return t < 10 ? OPX(t, xh + ((k - 4) >> 1)) : 0;
}
constexpr int vm_after(int hn) {
  int n = 0, last = 0;
  const int lastph = (hn == 0) ? -2 : hn - 2;
  for (int ph = -2; ph <= lastph; ++ph)
    for (int k = 0; k < 9; ++k) {
      const int op = sched_op(ph, k);
      if (!op) continue;
      ++n;
      if ((op >> 8) == 1 && ((op & 0xff) >> 2) == (hn >> 1)) last = n;
      if ((op >> 8) == 2 && ((op & 0xff) >> 1) == hn) last = n;
    }
  return n - last;
}

DI void linear_touch(const P1& p, unsigned scr, int part) {
  const int w = __builtin_amdgcn_readfirstlane(threadIdx.x >> 6), arr = w >> 1;
  const unsigned e = (w & 1) * 20 + ((threadIdx.x & 63) < 20 ? (threadIdx.x & 63) : 19);
  const char* base = (const char*)(arr == 0 ? p.x : arr == 1 ? p.wqkv : arr == 2 ? p.wg : p.wu);
  unsigned off;
  if (arr == 0) off = (blockIdx.x * 8 + e / 5) * 1280u + (part * 5 + e % 5) * 128u;
  else {
    const unsigned share = arr == 1 ? 4800u : 4320u;
    unsigned l = e * 128u; if (l > share - 4u) l = share - 4u;
    off = (part * 256 + blockIdx.x) * share + l;
  }
  touch4(base, off, scr);
}

template <int TYPE>
DI void k1_tile(const P1& p, char* smem, int rb, int ct, int idx STAMP_ARG) {
  constexpr int NT = (TYPE == 2) ? 6 : 5;
  constexpr int WSLOT = (TYPE == 2) ? 12288 : 10240;
  constexpr int WP = (TYPE == 2) ? 192 : 320;
  constexpr unsigned WPITCH = (TYPE == 2) ? F_ * 2 : NQKV * 2;
  const int tid = threadIdx.x, lane = tid & 63;
  const int wave = __builtin_amdgcn_readfirstlane(tid >> 6);
  const int wm = wave & 3, wn = wave >> 2;
  const int g = lane >> 4, c = lane & 15, q = (lane >> 2) & 3, pp = lane & 3;
  const unsigned lbase = lds_addr(smem);

  const char* xbase = (const char*)p.x + (size_t)rb * 256 * 1280;
  unsigned xoff;
  {
    const int row = wave * 8 + (lane >> 3);
    xoff = row * 1280 + (((lane & 7) ^ ((row >> 1) & 7)) << 4);
  }
  const char* wsrc[2];
  unsigned woff[2];
  if (TYPE == 2) {
    const int r = lane / 12, j0 = lane % 12;
    const int kr = 4 * wave + (r & 3);
    const int j = j0 ^ (((kr >> 3) & 1) << 1);
    wsrc[0] = (const char*)p.wg; wsrc[1] = (const char*)p.wu;
    woff[0] = woff[1] = kr * WPITCH + (ct * 96 + j * 8) * 2;
  } else {
    const int r = lane >= 20, j0 = (lane >= 40) ? 0 : lane - 20 * r;
#pragma unroll
    for (int i = 0; i < 2; ++i) {
      const int kr = 2 * (wave + 8 * i) + r;
      const int j = j0 ^ (((kr >> 3) & 1) << 1);
      int col;
      if (TYPE == 1) col = ct * 160 + j * 8;
      else { const int seg = j / 5, jj = j % 5; col = 640 + (seg & 1) * 640 + ct * 80 + (seg >> 1) * 40 + jj * 8; }
      wsrc[i] = (const char*)p.wqkv;
      woff[i] = kr * WPITCH + col * 2;
    }
  }
  auto issue_x1 = [&](int t, int i) {
    glds16(xbase + (size_t)(i * 81920 + t * 128), xoff, lbase + (t % 3) * XSLOT + wave * 1024 + i * 8192);
  };
  auto issue_w1 = [&](int h, int i) {
    glds16m(wsrc[i] + (size_t)h * 32 * WPITCH, woff[i], lbase + L_W + (h % 5) * WSLOT + wave * (WSLOT / 16) + i * (WSLOT / 2), (1ull << (WSLOT / 256)) - 1);
  };

  auto issue = [&](int op) {
    if ((op >> 8) == 1) issue_x1((op & 0xff) >> 2, op & 3);
    else if ((op >> 8) == 2) issue_w1((op & 0xff) >> 1, op & 1);
    else if ((op >> 8) == 3) linear_touch(p, lbase + LDS1 - 256, op & 1);
  };
  STAMPK(p.ws, 0, 0);
  const bool do_gate = (TYPE == 1) && (ct == 0);
  if (TYPE != 2) {
    constexpr int RAWB = (TYPE == 1) ? 6400 : 3200;
    if (wave < 7) {
      unsigned o = wave * 1024 + lane * 16; if (o > RAWB - 16) o = RAWB - 16;
      glds16((const char*)p.wphi + (size_t)ct * RAWB, o, lbase + L_RAW + wave * 1024);
    } else {
      glds16((const char*)p.gw, lane * 16, lbase + L_GW);
      glds16m((const char*)p.gw, 1024 + (lane & 15) * 16, lbase + L_GW + 1024, 0xffffull);
    }
  }
  static_for<9>([&](auto KC) { constexpr int op = sched_op(-2, decltype(KC)::value); issue(op); });
  if (TYPE != 2) {
    if (tid < 4) *(unsigned*)(smem + L_ZERO + tid * 4) = 0u;
    if (tid >= 64 && tid < 68) *(unsigned*)(smem + L_ONES + (tid - 64) * 4) = 0x3f803f80u;
  }

  int xrd[2];
  {
    const int f = (c >> 1) & 7;
    xrd[0] = (wm * 64 + c) * 128 + ((g ^ f) << 4);
    xrd[1] = (wm * 64 + c) * 128 + (((4 + g) ^ f) << 4);
  }
  int wrd[NT];
  {
    const int sw = (g & 1) << 5;
#pragma unroll
    for (int t = 0; t < NT; ++t) {
      if (TYPE == 2) {
        const int sub = t / 3, tt = wn * 3 + t % 3;
        wrd[t] = L_W + sub * 6144 + (8 * g + q) * 192 + ((tt * 32 + pp * 8) ^ sw);
      } else {
        wrd[t] = L_W + (8 * g + q) * 320 + ((wn * 160 + pp * 40 + t * 8) ^ sw);
      }
    }
  }

  f32x4 acc[4][NT];
#pragma unroll
  for (int m = 0; m < 4; ++m)
#pragma unroll
    for (int t = 0; t < NT; ++t) acc[m][t] = f32x4{0.f, 0.f, 0.f, 0.f};
  f32x4 gacc[4];
#pragma unroll
  for (int m = 0; m < 4; ++m) gacc[m] = f32x4{0.f, 0.f, 0.f, 0.f};
  const bool gate_wave = do_gate && (wn == 0);

  s16x8 xf[2][4], wf[2][NT];
  auto read_x = [&](int h, int b, int m) {
    xf[b][m] = *(const s16x8*)(smem + ((h >> 1) % 3) * XSLOT + xrd[h & 1] + m * 2048);
  };
  auto read_w = [&](int h, int b, int t) {
    const char* a = smem + wrd[t] + (h % 5) * WSLOT;
    wf[b][t] = cat(tr_read(a), tr_read(a + 4 * WP));
  };

  { constexpr int vmn = vm_after(0); WAIT_V(vmn); } WAIT_L0(); BAR();
  STAMPK(p.ws, 0, 1); STAMPC(p.ws, 0, 13);
#pragma unroll
  for (int m = 0; m < 4; ++m) read_x(0, 0, m);
#pragma unroll
  for (int t = 0; t < NT; ++t) read_w(0, 0, t);
  static_for<9>([&](auto KC) { constexpr int op = sched_op(-1, decltype(KC)::value); issue(op); });
  static_for<20>([&](auto HC) {
    constexpr int h = decltype(HC)::value;
    constexpr int b = h & 1;
    if constexpr (h + 1 < 20) { constexpr int vmn = vm_after(h + 1); WAIT_V(vmn); }
    WAIT_L0(); BAR();
    if (h < 8) STAMPK(p.ws, 0, 2 + (h < 8 ? h : 0));
    static_for<4>([&](auto MC) {
      constexpr int m = decltype(MC)::value;
      SCHED();
      if (h + 1 < 20) {
        if (m == 0) { read_w(h + 1, b ^ 1, 0); read_x(h + 1, b ^ 1, 0); read_w(h + 1, b ^ 1, 1); }
        if (m == 1) { read_w(h + 1, b ^ 1, 2); read_w(h + 1, b ^ 1, 3); read_x(h + 1, b ^ 1, 1); }
        if (m == 2) { read_w(h + 1, b ^ 1, 4); if (NT == 6) read_w(h + 1, b ^ 1, NT - 1); read_x(h + 1, b ^ 1, 2); read_x(h + 1, b ^ 1, 3); }
      }
#pragma unroll
      for (int t = 0; t < NT; ++t) acc[m][t] = mfma16(wf[b][t], xf[b][m], acc[m][t]);
      if (h + 1 < 20 && m < 3) {
#pragma unroll
        for (int i = 0; i < NT; ++i) {
          __builtin_amdgcn_sched_group_barrier(0x008, 1, 0);
          __builtin_amdgcn_sched_group_barrier(0x100, 1, 0);
        }
      }
      SCHED();
      constexpr int op0 = sched_op(h, 2 * m), op1 = sched_op(h, 2 * m + 1);
      issue(op0); issue(op1);
    });
    { constexpr int op8 = sched_op(h, 8); issue(op8); }
    if (TYPE == 1 && gate_wave) {
      s16x8 gf = *(const s16x8*)(smem + L_GW + h * 64 + g * 16);
#pragma unroll
      for (int m = 0; m < 4; ++m) gacc[m] = mfma16(gf, xf[b][m], gacc[m]);
    }
  });
  STAMPK(p.ws, 0, 11); STAMPC(p.ws, 0, 14);

  const int srow0 = rb * 256 + wm * 64;
  if (TYPE == 2) {
    constexpr float C0 = -2.0f * 0.7978845608028654f * 1.4426950408889634f, C1 = C0 * 0.044715f;
#pragma unroll
    for (int m = 0; m < 4; ++m) {
      unsigned hp[3][2];
#pragma unroll
      for (int t = 0; t < 3; ++t) {
        float h[4];
#pragma unroll
        for (int r = 0; r < 4; ++r) {
          const float xg = acc[m][t][r], up = acc[m][t + 3][r];
          const float e = fexp2(xg * __builtin_fmaf(xg * xg, C1, C0));
          h[r] = xg * frcp(1.0f + e) * up;
        }
        hp[t][0] = pack2(h[0], h[1]); hp[t][1] = pack2(h[2], h[3]);
      }
      const int s = srow0 + m * 16;
      char* hb = p.ws + WS_HA + ((size_t)(s >> 6) * KS_T * 4 + ((s >> 4) & 3)) * 1024 + lane * 16;
      *(u32x4*)(hb + (size_t)(ct * 3 + wn) * 4096) = u32x4{hp[0][0], hp[0][1], hp[1][0], hp[1][1]};
      *(u32x2*)(hb + (size_t)(ct * 3 + 2) * 4096 + wn * 8) = u32x2{hp[2][0], hp[2][1]};
    }
    return;
  }

  constexpr float L2E = 1.4426950408889634f;
  s16x8 af[5][3];
  {
    const int src = (TYPE == 1) ? pp : (pp & 1);
    const int hl = (TYPE == 1) ? wn * 4 + pp : wn * 2 + (pp & 1);
    const bool on = (g == src);
    const int a0 = on ? L_RAW + hl * 800 + q * 40 : L_ZERO;
    const int st = on ? 8 : 0, sd = on ? 160 : 0;
    const s16x4 z4 = {0, 0, 0, 0};
    const unsigned sg = (TYPE == 0 && c >= 8) ? 0x80008000u : 0u;
#pragma unroll
    for (int t = 0; t < 5; ++t) {
      af[t][0] = cat(tr_read(smem + a0 + t * st), tr_read(smem + a0 + t * st + sd));
      af[t][1] = cat(tr_read(smem + a0 + t * st + 2 * sd), tr_read(smem + a0 + t * st + 3 * sd));
      af[t][2] = cat(tr_read(smem + a0 + t * st + 4 * sd), z4);
      if (TYPE == 0) {
#pragma unroll
        for (int ks = 0; ks < 3; ++ks) af[t][ks] = __builtin_bit_cast(s16x8, __builtin_bit_cast(u32x4, af[t][ks]) ^ u32x4{sg, sg, sg, sg});
      }
    }
  }
#pragma unroll
  for (int m = 0; m < 4; ++m) {
    s16x8 bq[3];
    {
      u32x4 b0 = {pack2(acc[m][0][0], acc[m][0][1]), pack2(acc[m][0][2], acc[m][0][3]), pack2(acc[m][1][0], acc[m][1][1]), pack2(acc[m][1][2], acc[m][1][3])};
      u32x4 b1 = {pack2(acc[m][2][0], acc[m][2][1]), pack2(acc[m][2][2], acc[m][2][3]), pack2(acc[m][3][0], acc[m][3][1]), pack2(acc[m][3][2], acc[m][3][3])};
      u32x4 b2 = {pack2(acc[m][4][0], acc[m][4][1]), pack2(acc[m][4][2], acc[m][4][3]), 0u, 0u};
      bq[0] = __builtin_bit_cast(s16x8, b0); bq[1] = __builtin_bit_cast(s16x8, b1); bq[2] = __builtin_bit_cast(s16x8, b2);
    }
    f32x4 pr[5];
#pragma unroll
    for (int t = 0; t < 5; ++t) pr[t] = f32x4{0.f, 0.f, 0.f, 0.f};
#pragma unroll
    for (int ks = 0; ks < 3; ++ks)
#pragma unroll
      for (int t = 0; t < 5; ++t) pr[t] = mfma16(af[t][ks], bq[ks], pr[t]);
    const int s = srow0 + m * 16 + c;
    if (TYPE == 1) {
      const int head = ct * 8 + wn * 4 + g;
      u32x4* dst = (u32x4*)(p.ws + WS_PQ + ((size_t)head * S_ + s) * 48);
      dst[0] = u32x4{pack2(pr[0][0], pr[0][1]), pack2(pr[0][2], pr[0][3]), pack2(pr[1][0], pr[1][1]), pack2(pr[1][2], pr[1][3])};
      dst[1] = u32x4{pack2(pr[2][0], pr[2][1]), 0u, pack2(pr[2][2], pr[2][3]), pack2(pr[3][0], pr[3][1])};
      dst[2] = u32x4{pack2(pr[3][2], pr[3][3]), pack2(pr[4][0], pr[4][1]), pack2(pr[4][2], pr[4][3]), 0u};
      if (gate_wave && g == 0) {
        const float lg = gacc[m][0];
        ((float*)(p.ws + WS_G))[s] = frcp(1.0f + fexp2(-lg * L2E));
      }
    } else {
      float mx = pr[0][0];
#pragma unroll
      for (int t = 0; t < 5; ++t)
#pragma unroll
        for (int r = 0; r < 4; ++r) mx = fmaxf(mx, pr[t][r]);
      const float mxl = -mx * L2E;
      f32x2 e1[10], s2 = {0.f, 0.f};
#pragma unroll
      for (int t = 0; t < 5; ++t)
#pragma unroll
        for (int r = 0; r < 2; ++r) {
          const f32x2 a = __builtin_elementwise_fma(f32x2{pr[t][2 * r], pr[t][2 * r + 1]}, f32x2{L2E, L2E}, f32x2{mxl, mxl});
          const f32x2 e = {fexp2(a[0]), fexp2(a[1])};
          e1[t * 2 + r] = e; s2 += e;
        }
      const float i1 = frcp(s2[0] + s2[1]);
      unsigned pk1[10];
#pragma unroll
      for (int j = 0; j < 10; ++j) { const f32x2 v = e1[j] * f32x2{i1, i1}; pk1[j] = pack2(v[0], v[1]); }
      char* rowp = smem + (wm * 64 + m * 16 + c) * IMG_PITCH;
      char* pd = rowp + (wn * 2 + (g & 1)) * 80 + (g >> 1) * 40;
#pragma unroll
      for (int j = 0; j < 5; ++j) *(u32x2*)(pd + j * 8) = u32x2{pk1[2 * j], pk1[2 * j + 1]};
      if (g >= 2) {
        char* vd = rowp + 320 + (wn * 2 + (g - 2)) * 40;
        const u32x4 b0 = __builtin_bit_cast(u32x4, bq[0]), b1 = __builtin_bit_cast(u32x4, bq[1]), b2 = __builtin_bit_cast(u32x4, bq[2]);
        *(u32x2*)(vd + 0) = u32x2{b0[0], b0[1]};
        *(u32x2*)(vd + 8) = u32x2{b0[2], b0[3]};
        *(u32x2*)(vd + 16) = u32x2{b1[0], b1[1]};
        *(u32x2*)(vd + 24) = u32x2{b1[2], b1[3]};
        *(u32x2*)(vd + 32) = u32x2{b2[0], b2[1]};
      }
    }
  }
  if (TYPE == 0) {
    WAIT_L0(); BAR();
    STAMPK(p.ws, 0, 10);
    const int hh = wave & 3, u = wave >> 2;
    const int abase = (8 * g + q) * IMG_PITCH + hh * 80;
    const int a0 = abase + pp * 8, a1 = abase + 32 + pp * 8, a2 = abase + 64 + (pp & 1) * 8;
    int b0, bstep;
    if (u == 0) { b0 = (8 * g + q) * IMG_PITCH + 320 + hh * 40 + pp * 8; bstep = IMG_PITCH; }
    else if (pp != 1) { b0 = (8 * g + q) * IMG_PITCH + 320 + hh * 40 + 32; bstep = IMG_PITCH; }
    else { b0 = L_ONES; bstep = 0; }
    f32x4 kv[3];
#pragma unroll
    for (int a = 0; a < 3; ++a) kv[a] = f32x4{0.f, 0.f, 0.f, 0.f};
#pragma unroll
    for (int ks = 0; ks < 8; ++ks) {
      const int ro = ks * 32 * IMG_PITCH;
      s16x8 bf = cat(tr_read(smem + b0 + ks * 32 * bstep), tr_read(smem + b0 + (ks * 32 + 4) * bstep));
      s16x8 f0 = cat(tr_read(smem + a0 + ro), tr_read(smem + a0 + ro + 4 * IMG_PITCH));
      s16x8 f1 = cat(tr_read(smem + a1 + ro), tr_read(smem + a1 + ro + 4 * IMG_PITCH));
      s16x8 f2 = cat(tr_read(smem + a2 + ro), tr_read(smem + a2 + ro + 4 * IMG_PITCH));
      kv[0] = mfma16(f0, bf, kv[0]);
      kv[1] = mfma16(f1, bf, kv[1]);
      kv[2] = mfma16(f2, bf, kv[2]);
    }
    const int head = ct * 4 + hh;
    const int d = 16 * u + c;
    if (d <= 20) {
      float* sl = (float*)(p.ws + WS_SLAB) + ((size_t)(rb * 32 + head) * 21 + d) * 40 + 4 * g;
      *(f32x4*)(sl) = kv[0];
      *(f32x4*)(sl + 16) = kv[1];
      if (g < 2) *(f32x4*)(sl + 32) = kv[2];
    }
  }
}

__global__ __launch_bounds__(512, 2) void k1(P1 p) {
  __shared__ __attribute__((aligned(1024))) char smem[LDS1];
  STAMP_DECL;
  {
    const unsigned long long pc = __builtin_amdgcn_s_getpc() & ~255ull;
    unsigned o = threadIdx.x * 128u; if (o > K1_CODE_BYTES - 128u) o = K1_CODE_BYTES - 128u;
    touch4((const void*)pc, o, lds_addr(smem) + LDS1 - 256);
  }
  const int bid = blockIdx.x;
  const int xcd = bid & 7, idx = bid >> 3;
  const int rb = (xcd >> 2) * 4 + (idx & 3);
  const int slot = (idx >> 2) * 4 + (xcd & 3);
  if (slot < 30) {
    if (slot < 8) k1_tile<0>(p, smem, rb, slot, idx STAMP_PASS);
    else if (slot < 12) k1_tile<1>(p, smem, rb, slot - 8, idx STAMP_PASS);
    else k1_tile<2>(p, smem, rb, slot - 12, idx STAMP_PASS);
    STAMPK(p.ws, 0, 12); STAMP_DUMP(p.ws, 0);
  } else {
    const int lane = threadIdx.x & 63, wave = __builtin_amdgcn_readfirstlane(threadIdx.x >> 6);
    const int sp = rb * 2 + (slot - 30);
    STAMPK(p.ws, 0, 0);
    linear_touch(p, lds_addr(smem) + LDS1 - 256, 0);
    __builtin_amdgcn_s_sleep(48);
    linear_touch(p, lds_addr(smem) + LDS1 - 256, 1);
    for (int i = 0; i < SPDELAY; ++i) __builtin_amdgcn_s_sleep(127);
    const int u0 = sp * 19, u1 = (u0 + 19 < 296) ? u0 + 19 : 296;
    for (int u = u0; u < u0 + 10; ++u) pretile_issue(p, lds_addr(smem) + (u - u0) * 10240, u, wave, lane);
    for (int u = u0 + 10; u < u0 + 15 && u < u1; ++u) pretile_issue(p, lds_addr(smem) + (u - u0) * 10240, u, wave, lane);
    for (int r = 0; r < 2; ++r) {
      char* dst = p.ws + WS_HA + ((size_t)(2 * sp + r) * KS_T + KS_H) * 4096;
      for (int e = threadIdx.x; e < KS_O * 4096 / 16; e += 512) *(u32x4*)(dst + e * 16) = u32x4{~0u, ~0u, ~0u, ~0u};
    }
    WAIT_V(0); BAR();
    for (int u = u0; u < u0 + 4; ++u) pretile_finish(p, smem + (u - u0) * 10240, u, wave, lane);
    WAIT_L0(); BAR();
    for (int u = u0 + 15; u < u1; ++u) pretile_issue(p, lds_addr(smem) + (u - u0 - 15) * 10240, u, wave, lane);
    for (int u = u0 + 4; u < u0 + 15 && u < u1; ++u) pretile_finish(p, smem + (u - u0) * 10240, u, wave, lane);
    WAIT_V(0); BAR();
    for (int u = u0 + 15; u < u1; ++u) pretile_finish(p, smem + (u - u0 - 15) * 10240, u, wave, lane);
    STAMPK(p.ws, 0, 12); STAMP_DUMP(p.ws, 0);
  }
}

struct P2 { char* ws; u16* out; };
constexpr int L2_RED = 0;
constexpr int L2_KV = 61440;
constexpr int LDS2 = L2_KV + 4 * 32 * 144;
#define DEPTH 3

__global__ __launch_bounds__(512, 2) void k2(P2 p) {
  __shared__ __attribute__((aligned(1024))) char smem[LDS2];
  STAMP_DECL;
  if (threadIdx.x < 128) {
    const unsigned long long pc = __builtin_amdgcn_s_getpc() & ~255ull;
    unsigned o = threadIdx.x * 128u; if (o > K2_CODE_BYTES - 128u) o = K2_CODE_BYTES - 128u;
    touch4((const void*)pc, o, lds_addr(smem));
  }
  const int bid = blockIdx.x;
  const int xcd = bid & 7, idx = bid >> 3;
  const int rb64 = xcd * 4 + (idx >> 3), cb = idx & 7;
  STAMPK(p.ws, 1, 8);
  const int tid = threadIdx.x, lane = tid & 63;
  const int wave = __builtin_amdgcn_readfirstlane(tid >> 6);
  const int g = lane >> 4, c = lane & 15;
  const int l16 = lane * 16;

  if (wave < 4) {
    const auto rsA = __builtin_amdgcn_make_buffer_rsrc((void*)(p.ws + WS_HA + (size_t)rb64 * KS_T * 4096), 0, KS_T * 4096, 0x00020000);
    const auto rsB = __builtin_amdgcn_make_buffer_rsrc((void*)(p.ws + WS_WB + (size_t)cb * KS_T * 5120), 0, KS_T * 5120, 0x00020000);
    s16x8 af[DEPTH][4], bf[DEPTH][5];
    f32x4 acc[4][5];
#pragma unroll
    for (int m = 0; m < 4; ++m)
#pragma unroll
      for (int n = 0; n < 5; ++n) acc[m][n] = f32x4{0.f, 0.f, 0.f, 0.f};
    auto load_a = [&](int i, int slot) {
      if (i < 14) {
        int ks = wave + 4 * i; if (ks > KS_H - 1) ks = KS_H - 1;
#pragma unroll
        for (int m = 0; m < 4; ++m) af[slot][m] = __builtin_bit_cast(s16x8, __builtin_amdgcn_raw_buffer_load_b128(rsA, l16 + m * 1024, ks * 4096, 0));
      } else if (i < 18) {
#pragma unroll
        for (int m = 0; m < 4; ++m) af[slot][m] = __builtin_bit_cast(s16x8, __builtin_amdgcn_raw_buffer_load_b128(rsA, l16 + m * 1024, (KS_H + wave + 4 * (i - 14)) * 4096, 16));
      } else {
#pragma unroll
        for (int m = 0; m < 4; ++m) {
          const u32x2 lo = __builtin_amdgcn_raw_buffer_load_b64(rsA, lane * 8 + m * 1024, (KS_H + 16 + wave) * 4096, 16);
          const u32x2 hi = __builtin_amdgcn_raw_buffer_load_b64(rsA, lane * 8 + 512 + m * 1024, (KS_H + 16 + wave) * 4096, 16);
          af[slot][m] = __builtin_bit_cast(s16x8, u32x4{lo[0], lo[1], hi[0], hi[1]});
        }
      }
    };
    auto load_b = [&](int i, int slot) {
      int ks = (i < 14) ? wave + 4 * i : KS_H + wave + 4 * (i - 14);
      if (i < 14 && ks > KS_H - 1) ks = KS_H - 1;
#pragma unroll
      for (int n = 0; n < 5; ++n) bf[slot][n] = __builtin_bit_cast(s16x8, __builtin_amdgcn_raw_buffer_load_b128(rsB, l16 + (n & 3) * 1024, ks * 5120 + (n >> 2) * 4096, 0));
    };
    auto missing = [&](int slot) {
      unsigned bad = 0u;
#pragma unroll
      for (int m = 0; m < 4; ++m) {
        const u32x4 w = __builtin_bit_cast(u32x4, af[slot][m]);
        bad |= (w[0] == ~0u) | (w[1] == ~0u) | (w[2] == ~0u) | (w[3] == ~0u);
      }
      return __any(bad);
    };
    float gm[4];
#pragma unroll
    for (int m = 0; m < 4; ++m) gm[m] = ((const float*)(p.ws + WS_G))[rb64 * 64 + m * 16 + c];
#pragma unroll
    for (int i = 0; i < DEPTH; ++i) { load_b(i, i); load_a(i, i); SCHED(); }
    const bool full = wave < 2;
#pragma unroll
    for (int i = 0; i < 19; ++i) {
      const int slot = i % DEPTH;
      if (i == 1) STAMPK(p.ws, 1, 0);
      if (i == 14) {
        STAMPK(p.ws, 1, 4);
#pragma unroll
        for (int m = 0; m < 4; ++m) {
          const float sc = 1.0f - gm[m];
#pragma unroll
          for (int n = 0; n < 5; ++n) acc[m][n] *= sc;
        }
      }
      if (i >= 14) {
        if (__builtin_expect(missing(slot), 0)) {
          unsigned spins = 0;
          do {
            __builtin_amdgcn_s_sleep(2);
            load_a(i, slot);
          } while (missing(slot) && ++spins < (1u << 20));
        }
      }
      if (i != 13 || full) {
#pragma unroll
        for (int m = 0; m < 4; ++m)
#pragma unroll
          for (int n = 0; n < 5; ++n) acc[m][n] = mfma16(bf[slot][n], af[slot][m], acc[m][n]);
      }
      SCHED();
      if (i + DEPTH < 19) { load_b(i + DEPTH, slot); load_a(i + DEPTH, slot); }
      SCHED();
    }
    STAMPK(p.ws, 1, 5);
    auto tail = [&](auto WC) {
      constexpr int W = decltype(WC)::value;
#pragma unroll
      for (int m = 0; m < 4; ++m) {
        if (m != W) {
          const int sl = (W - m - 1) & 3;
#pragma unroll
          for (int n = 0; n < 5; ++n) *(f32x4*)(smem + L2_RED + ((m * 3 + sl) * 5 + n) * 1024 + l16) = acc[m][n];
        }
      }
      WAIT_L0(); BAR();
      STAMPK(p.ws, 1, 6);
      f32x4 r[5];
#pragma unroll
      for (int n = 0; n < 5; ++n) r[n] = acc[W][n];
#pragma unroll
      for (int sl = 0; sl < 3; ++sl)
#pragma unroll
        for (int n = 0; n < 5; ++n) r[n] += *(const f32x4*)(smem + L2_RED + ((W * 3 + sl) * 5 + n) * 1024 + l16);
      u16* o = p.out + (size_t)(rb64 * 64 + W * 16 + c) * D_ + cb * 80 + 4 * g;
#pragma unroll
      for (int n = 0; n < 5; ++n) *(u32x2*)(o + n * 16) = u32x2{pack2(r[n][0], r[n][1]), pack2(r[n][2], r[n][3])};
    };
    if (wave == 0) tail(std::integral_constant<int, 0>{});
    else if (wave == 1) tail(std::integral_constant<int, 1>{});
    else if (wave == 2) tail(std::integral_constant<int, 2>{});
    else tail(std::integral_constant<int, 3>{});
    STAMPK(p.ws, 1, 7); STAMP_DUMP(p.ws, 1);
    return;
  }

  {
    constexpr float L2E = 1.4426950408889634f;
    __builtin_amdgcn_s_setprio(3);
    const int hw = wave - 4, head = idx;
    char* kvim = smem + L2_KV + hw * (32 * 144);
    f32x4 sl[4][8];
#pragma unroll
    for (int k = 0; k < 4; ++k) {
      int e = k * 64 + lane; if (e > 209) e = 209;
#pragma unroll
      for (int r = 0; r < 8; ++r) sl[k][r] = *(const f32x4*)(p.ws + WS_SLAB + (size_t)(r * 32 + head) * 3360 + e * 16);
    }
    unsigned pw[4][5];
    float gv[4];
#pragma unroll
    for (int i = 0; i < 4; ++i) {
      const int s = xcd * 256 + (hw * 4 + i) * 16 + c;
      const char* src = p.ws + WS_PQ + ((size_t)head * S_ + s) * 48 + (g & 1) * 24;
      const u32x2 a = *(const u32x2*)(src), b = *(const u32x2*)(src + 8);
      pw[i][0] = a[0]; pw[i][1] = a[1]; pw[i][2] = b[0]; pw[i][3] = b[1];
      pw[i][4] = *(const unsigned*)(src + 16);
      gv[i] = ((const float*)(p.ws + WS_G))[s];
    }
    SCHED();
    for (int e = lane; e < 32 * 144 / 16; e += 64) *(u32x4*)(kvim + e * 16) = u32x4{0u, 0u, 0u, 0u};
#pragma unroll
    for (int k = 0; k < 4; ++k) {
      const int e = k * 64 + lane;
      const f32x4 a = ((sl[k][0] + sl[k][1]) + (sl[k][2] + sl[k][3])) + ((sl[k][4] + sl[k][5]) + (sl[k][6] + sl[k][7]));
      const int d = e / 10, f = (e % 10) * 4;
      if (e < 210) {
        *(unsigned*)(kvim + d * 144 + (16 * (f / 10) + f % 10) * 2) = pack2(a[0], a[1]);
        *(unsigned*)(kvim + d * 144 + (16 * ((f + 2) / 10) + (f + 2) % 10) * 2) = pack2(a[2], a[3]);
      }
    }
    STAMPK(p.ws, 1, 0);
    s16x8 pf[4][2];
    const unsigned sg = (g >= 2) ? 0x80008000u : 0u;
#pragma unroll
    for (int i = 0; i < 4; ++i) {
      f32x2 v[5];
#pragma unroll
      for (int j = 0; j < 5; ++j) { const unsigned w = pw[i][j] ^ sg; v[j] = f32x2{__uint_as_float(w << 16), __uint_as_float(w & 0xffff0000u)}; }
      float mx = v[0][0];
#pragma unroll
      for (int j = 0; j < 5; ++j) mx = fmaxf(fmaxf(mx, v[j][0]), v[j][1]);
      { const auto r = __builtin_amdgcn_permlane16_swap(__float_as_uint(mx), __float_as_uint(mx), false, false);
        const unsigned r0 = r[0], r1 = r[1]; mx = fmaxf(__uint_as_float(r0), __uint_as_float(r1)); }
      const float mxl = -mx * L2E;
      f32x2 s2 = {0.f, 0.f};
#pragma unroll
      for (int j = 0; j < 5; ++j) {
        const f32x2 a = __builtin_elementwise_fma(v[j], f32x2{L2E, L2E}, f32x2{mxl, mxl});
        v[j] = f32x2{fexp2(a[0]), fexp2(a[1])}; s2 += v[j];
      }
      float sm = s2[0] + s2[1];
      { const auto r = __builtin_amdgcn_permlane16_swap(__float_as_uint(sm), __float_as_uint(sm), false, false);
        const unsigned r0 = r[0], r1 = r[1]; sm = __uint_as_float(r0) + __uint_as_float(r1); }
      const float inv = frcp(sm);
      unsigned pk[5];
#pragma unroll
      for (int j = 0; j < 5; ++j) { const f32x2 t = v[j] * f32x2{inv, inv}; pk[j] = pack2(t[0], t[1]); }
      pf[i][0] = __builtin_bit_cast(s16x8, u32x4{pk[0], pk[1], pk[2], pk[3]});
      pf[i][1] = __builtin_bit_cast(s16x8, u32x4{pk[4], 0u, 0u, 0u});
    }
    STAMPK(p.ws, 1, 1);
    s16x8 kf[3][2];
    {
      const int gp = c >> 2, r = c & 3;
      const int d0 = (gp < 3 ? gp : 2) * 8 + r, d1 = (gp & 1) * 8 + 4 + r;
#pragma unroll
      for (int ks = 0; ks < 2; ++ks) {
        kf[0][ks] = *(const s16x8*)(kvim + d0 * 144 + g * 32 + ks * 16);
        kf[1][ks] = *(const s16x8*)(kvim + d1 * 144 + g * 32 + ks * 16);
        kf[2][ks] = *(const s16x8*)(kvim + 20 * 144 + g * 32 + ks * 16);
      }
    }
    u32x4 ov[4];
#pragma unroll
    for (int i = 0; i < 4; ++i) {
      f32x4 n0 = {0.f, 0.f, 0.f, 0.f}, n1 = n0, dn = n0;
#pragma unroll
      for (int ks = 0; ks < 2; ++ks) {
        n0 = mfma16(kf[0][ks], pf[i][ks], n0);
        n1 = mfma16(kf[1][ks], pf[i][ks], n1);
        dn = mfma16(kf[2][ks], pf[i][ks], dn);
      }
      const float sc = gv[i] / (dn[0] + 1e-6f);
      u32x4 v = {pack2(n0[0] * sc, n0[1] * sc), pack2(n0[2] * sc, n0[3] * sc), pack2(n1[0] * sc, n1[1] * sc), pack2(n1[2] * sc, n1[3] * sc)};
#pragma unroll
      for (int j = 0; j < 4; ++j) if (v[j] == ~0u) v[j] = 0x7fc07fc0u;
      ov[i] = v;
    }
    SCHED();
    const auto rsO = __builtin_amdgcn_make_buffer_rsrc((void*)(p.ws + WS_HA + (size_t)(xcd * 4 + hw) * KS_T * 4096), 0, KS_T * 4096, 0x00020000);
    if (g < 2) {
#pragma unroll
      for (int i = 0; i < 4; ++i)
        __builtin_amdgcn_raw_buffer_store_b128(ov[i], rsO, i * 1024 + ((2 * (head & 1) + g) * 16 + c) * 16, (KS_H + (head >> 1)) * 4096, 16);
    } else if (g == 2) {
#pragma unroll
      for (int i = 0; i < 4; ++i)
        __builtin_amdgcn_raw_buffer_store_b64(u32x2{ov[i][0], ov[i][1]}, rsO, i * 1024 + (head & 1) * 512 + (((head & 7) >> 1) * 16 + c) * 8, (KS_H + 16 + (head >> 3)) * 4096, 16);
    }
    STAMPK(p.ws, 1, 2); STAMP_DUMP(p.ws, 1);
  }
}

extern "C" void kernel_launch(void* const* d_in, const int* in_sizes, int n_in,
                              void* d_out, int out_size, void* d_ws, size_t ws_size,
                              hipStream_t stream) {
  P1 p1;
  p1.x = (const u16*)d_in[0]; p1.wqkv = (const u16*)d_in[1]; p1.wphi = (const u16*)d_in[2]; p1.wo = (const u16*)d_in[3];
  p1.wg = (const u16*)d_in[5]; p1.wu = (const u16*)d_in[6]; p1.wd = (const u16*)d_in[7]; p1.gw = (const u16*)d_in[8];
  p1.ws = (char*)d_ws;
  k1<<<256, 512, 0, stream>>>(p1);
  P2 p2; p2.ws = (char*)d_ws; p2.out = (u16*)d_out;
  k2<<<256, 512, 0, stream>>>(p2);
}
```

```cpp
#include <hip/hip_runtime.h>
#include <hip/hip_bf16.h>
#include <stdint.h>
#include <type_traits>
#include <utility>
#define STAMPK(ws, kern, i) do {} while (0)
#define STAMPC(ws, kern, i) do {} while (0)
#define STAMP_DECL
#define STAMP_DUMP(ws, kern) do {} while (0)
#define STAMP_ARG
#define STAMP_PASS

typedef __attribute__((ext_vector_type(8))) short s16x8;
typedef __attribute__((ext_vector_type(4))) short s16x4;
typedef __attribute__((ext_vector_type(8))) __bf16 bf16x8;
typedef __attribute__((ext_vector_type(2))) __bf16 bf16x2;
typedef __attribute__((ext_vector_type(4))) float f32x4;
typedef __attribute__((ext_vector_type(2))) float f32x2;
typedef __attribute__((ext_vector_type(4))) unsigned u32x4;
typedef __attribute__((ext_vector_type(2))) unsigned u32x2;
typedef unsigned short u16;
#define GAS __attribute__((address_space(1)))
#define LAS __attribute__((address_space(3)))
#define DI __device__ __forceinline__

constexpr int S_ = 2048, D_ = 640, F_ = 1728, NQKV = 1920;
constexpr int KS_H = 54, KS_O = 20, KS_T = 74;

constexpr size_t WS_HA   = 0;
constexpr size_t WS_WB   = WS_HA + (size_t)32 * KS_T * 4096;
constexpr size_t WS_PQ   = WS_WB + (size_t)8 * KS_T * 5120;
constexpr size_t WS_SLAB = WS_PQ + (size_t)32 * 2048 * 48;
constexpr size_t WS_G    = WS_SLAB + (size_t)32 * 8 * 3360;
constexpr size_t WS_FLAG = WS_G + 2048 * 4;
constexpr int FL_S = 0;
constexpr int FL_E = 256;
constexpr int FL_D = 320;
static_assert(WS_FLAG % 128 == 0, "flags");
DI f32x4 mfma16(s16x8 a, s16x8 b, f32x4 c) {
  return __builtin_amdgcn_mfma_f32_16x16x32_bf16(__builtin_bit_cast(bf16x8, a), __builtin_bit_cast(bf16x8, b), c, 0, 0, 0);
}
DI f32x4 mfma16h(s16x4 a, s16x4 b, f32x4 c) {
  const s16x4 z = {0, 0, 0, 0};
  return mfma16(__builtin_shufflevector(a, z, 0, 1, 2, 3, 4, 5, 6, 7), __builtin_shufflevector(b, z, 0, 1, 2, 3, 4, 5, 6, 7), c);
}
DI unsigned pack2(float a, float b) {
  bf16x2 v = {(__bf16)a, (__bf16)b};
  return __builtin_bit_cast(unsigned, v);
}
DI s16x4 tr_read(const char* lds) { return __builtin_amdgcn_ds_read_tr16_b64_v4i16((LAS s16x4*)(lds)); }
DI s16x8 cat(s16x4 a, s16x4 b) { return __builtin_shufflevector(a, b, 0, 1, 2, 3, 4, 5, 6, 7); }
DI void glds16(const void* sbase, unsigned voff, unsigned ldsaddr) {
  asm volatile("s_mov_b32 m0, %2\n\ts_nop 0\n\tglobal_load_lds_dwordx4 %0, %1" :: "v"(voff), "s"(sbase), "s"(ldsaddr) : "memory");
}
DI void glds16_sc1(const void* sbase, unsigned voff, unsigned ldsaddr) {
  asm volatile("s_mov_b32 m0, %2\n\ts_nop 0\n\tglobal_load_lds_dwordx4 %0, %1 sc1" :: "v"(voff), "s"(sbase), "s"(ldsaddr) : "memory");
}
DI void glds16m(const void* sbase, unsigned voff, unsigned ldsaddr, unsigned long long mask) {
  asm volatile("s_mov_b64 exec, %3\n\ts_mov_b32 m0, %2\n\ts_nop 0\n\tglobal_load_lds_dwordx4 %0, %1\n\ts_mov_b64 exec, -1"
               :: "v"(voff), "s"(sbase), "s"(ldsaddr), "s"(mask) : "memory");
}
DI void touch4(const void* sbase, unsigned voff, unsigned ldsaddr) {
  asm volatile("s_mov_b32 m0, %2\n\ts_nop 0\n\tglobal_load_lds_dword %0, %1" :: "v"(voff), "s"(sbase), "s"(ldsaddr) : "memory");
}
DI unsigned lds_addr(const char* p) { return (unsigned)(size_t)(const LAS char*)p; }
DI float fexp2(float x) { return __builtin_amdgcn_exp2f(x); }
DI float max3f(float a, float b, float c) { return __builtin_elementwise_maximum(__builtin_elementwise_maximum(a, b), c); }
DI unsigned umax(unsigned a, unsigned b) { return a > b ? a : b; }
DI unsigned nz(unsigned v) { return v < 0xfffffffeu ? v : 0xfffffffeu; }
DI float frcp(float x) { return __builtin_amdgcn_rcpf(x); }
#define WAIT_V(n) asm volatile("s_waitcnt vmcnt(%0)" :: "n"(n) : "memory")
#define WAIT_L0() asm volatile("s_waitcnt lgkmcnt(0)" ::: "memory")
#define BAR() __builtin_amdgcn_s_barrier()
#define SCHED() __builtin_amdgcn_sched_barrier(0)
#define FENCE() asm volatile("" ::: "memory")
template <class F, int... I> DI void static_for_impl(F&& f, std::integer_sequence<int, I...>) { (f(std::integral_constant<int, I>{}), ...); }
template <int N, class F> DI void static_for(F&& f) { static_for_impl(f, std::make_integer_sequence<int, N>{}); }
template <int V> using IC = std::integral_constant<int, V>;

DI unsigned gld(const GAS unsigned* p) { return __hip_atomic_load(p, __ATOMIC_RELAXED, __HIP_MEMORY_SCOPE_AGENT); }
DI void gst(GAS unsigned* p, unsigned v) { __hip_atomic_store(p, v, __ATOMIC_RELAXED, __HIP_MEMORY_SCOPE_AGENT); }
DI unsigned lds_ld(const char* p) { return __hip_atomic_load((const LAS unsigned*)p, __ATOMIC_RELAXED, __HIP_MEMORY_SCOPE_WORKGROUP); }
DI void lds_st(char* p, unsigned v) { __hip_atomic_store((LAS unsigned*)p, v, __ATOMIC_RELAXED, __HIP_MEMORY_SCOPE_WORKGROUP); }
DI void lds_addf(char* p, float v) { LAS float* q = (LAS float*)p; __hip_atomic_fetch_add(q, v, __ATOMIC_RELAXED, __HIP_MEMORY_SCOPE_WORKGROUP); }
DI void lds_or(char* p, unsigned v) { LAS unsigned* q = (LAS unsigned*)p; __hip_atomic_fetch_or(q, v, __ATOMIC_RELAXED, __HIP_MEMORY_SCOPE_WORKGROUP); }
DI unsigned lds_add(char* p, unsigned v) { return __hip_atomic_fetch_add((LAS unsigned*)p, v, __ATOMIC_RELAXED, __HIP_MEMORY_SCOPE_WORKGROUP); }

struct Prm {
  const u16* x; const u16* wqkv; const u16* wphi; const u16* wo;
  const u16* wg; const u16* wu; const u16* wd; const u16* gw;
  char* ws; u16* out;
};

constexpr int XSLOT = 32768;
constexpr int L_W = 3 * XSLOT;
constexpr int LDS_BYTES = 163840;
constexpr int L_AUX = L_W + 5 * 10240;
constexpr int L_RAW = L_AUX;
constexpr int L_GW = L_RAW + 7168;
constexpr int L_ZERO = L_GW + 2048;
constexpr int L_ONES = L_ZERO + 16;
constexpr int L_CTL = LDS_BYTES - 256;
constexpr int L_SCR = L_CTL - 256;
static_assert(L_ONES + 16 <= L_SCR && L_W + 5 * 12288 <= L_SCR, "lds");
constexpr int IMG_PITCH = 488;
constexpr int BS = 5120;
constexpr int P_KVOP = 8 * 3 * BS;
constexpr int C_CUR = L_CTL, C_OFF = L_CTL + 8, C_SIG = L_CTL + 16, C_CNT = L_CTL + 20, C_RQ = L_CTL + 24, C_KV = L_CTL + 28, C_RG = L_CTL + 32, C_PK = L_CTL + 48;
constexpr unsigned SPIN_CAP = 1u << 16;

#define CODE_BYTES 65536u
#define DAH 4
#define PSL 12
#define SPDELAY 1
constexpr int OPX(int t, int i) { return 0x100 | (t << 2) | i; }
constexpr int OPW(int h, int i) { return 0x200 | (h << 1) | i; }
constexpr int sched_op(int ph, int k) {
  if (ph == -2) { const int a[9] = {OPX(0, 0), OPX(0, 1), OPX(0, 2), OPX(0, 3), OPW(0, 0), OPW(0, 1), OPW(1, 0), OPW(1, 1), 0x300}; return k < 9 ? a[k] : 0; }
  if (ph == 1 && k == 8) return 0x301;
  if (ph == -1) { const int a[6] = {OPX(1, 0), OPX(1, 1), OPX(1, 2), OPX(1, 3), OPW(2, 0), OPW(2, 1)}; return k < 6 ? a[k] : 0; }
  if (ph == 0) { const int a[8] = {OPW(3, 0), OPW(3, 1), OPX(2, 0), OPX(2, 1), OPX(2, 2), OPX(2, 3), OPW(4, 0), OPW(4, 1)}; return k < 8 ? a[k] : 0; }
  if (ph == 1) { const int a[8] = {OPW(5, 0), OPW(5, 1), OPW(6, 0), OPW(6, 1), OPX(3, 0), 0, OPX(3, 1), 0}; return k < 8 ? a[k] : 0; }
  if (k >= 8 || (k & 1)) return 0;
  if (k < 4) return (ph + 5 < 20) ? OPW(ph + 5, k >> 1) : 0;
  const int t = (ph & 1) ? (ph + 5) / 2 : (ph + 4) / 2, xh = (ph & 1) ? 0 : 2;
  return t < 10 ? OPX(t, xh + ((k - 4) >> 1)) : 0;
}
constexpr int vm_after(int hn) {
  int n = 0, last = 0;
  const int lastph = (hn == 0) ? -2 : hn - 2;
  for (int ph = -2; ph <= lastph; ++ph)
    for (int k = 0; k < 9; ++k) {
      const int op = sched_op(ph, k);
      if (!op) continue;
      ++n;
      if ((op >> 8) == 1 && ((op & 0xff) >> 2) == (hn >> 1)) last = n;
      if ((op >> 8) == 2 && ((op & 0xff) >> 1) == hn) last = n;
    }
  return n - last;
}

DI int pt_krow(int ks, int slot) {
  const int g = slot >> 3, j = slot & 7;
  if (ks < KS_H) {
    const int G = ks / 3, e = ks % 3;
    return 96 * G + (e < 2 ? 48 * e + 16 * (j >> 2) + 4 * g + (j & 3) : 48 * (g >> 1) + 32 + 8 * (g & 1) + j);
  }
  const int k = ks - KS_H;
  if (k < 16) return (2 * k + (g >> 1)) * 20 + (g & 1) * 8 + j;
  return (8 * (k - 16) + 2 * g + (j >> 2)) * 20 + 16 + (j & 3);
}
DI void pretile_issue(const Prm& p, unsigned ldsbase, int unit, int wave, int lane) {
  const int ks = unit >> 2, q4 = unit & 3;
  const u16* src = (ks < KS_H) ? p.wd : p.wo;
#pragma unroll
  for (int i = 0; i < 2; ++i) {
    const int n = wave + 8 * i;
    const int slot = 2 * n + (lane >= 20), j = lane >= 20 ? lane - 20 : lane;
    const int kr = pt_krow(ks, slot & 31);
    glds16m(src, (unsigned)(kr * D_ + q4 * 160 + (j < 20 ? j : 0) * 8) * 2u, ldsbase + n * 640, (1ull << 40) - 1);
  }
}
DI void pretile_finish(const Prm& p, const char* img, int unit, int wave, int lane) {
  const int ks = unit >> 2, q4 = unit & 3;
  const int g = lane >> 4, q = (lane >> 2) & 3, pp = lane & 3;
  const auto rsW = __builtin_amdgcn_make_buffer_rsrc((void*)(p.ws + WS_WB), 0, 8 * KS_T * 5120, 0x00020000);
#pragma unroll
  for (int i = 0; i < 2; ++i) {
    const int n = wave + 8 * i;
    if (n < 10) {
      const char* a = img + (8 * g + q) * 320 + n * 32 + pp * 8;
      const s16x8 v = cat(tr_read(a), tr_read(a + 4 * 320));
      const int cb = 2 * q4 + (n >= 5), nt = n >= 5 ? n - 5 : n;
      __builtin_amdgcn_raw_buffer_store_b128(__builtin_bit_cast(u32x4, v), rsW, ((cb * KS_T + ks) * 5 + nt) * 1024 + lane * 16, 0, 16);
    }
  }
}

struct Ctx {
  char* smem; unsigned lbase;
  int tid, lane, wave;
  const char* tbase; unsigned toff, tstep;
};

template <int TYPE, class Early, class Ready, class Sig>
DI void gemm1_tile(const Prm& p, const Ctx& cx, int rb, int ct, Early&& early_load, Ready&& setup_ready, Sig&& signal STAMP_ARG) {
  constexpr int NT = (TYPE == 2) ? 6 : 5;
  constexpr int WSLOT = (TYPE == 2) ? 12288 : 10240;
  constexpr int WP = (TYPE == 2) ? 192 : 320;
  constexpr unsigned WPITCH = (TYPE == 2) ? F_ * 2 : NQKV * 2;
  constexpr float L2E = 1.4426950408889634f;
  char* smem = cx.smem;
  const unsigned lbase = cx.lbase;
  const int tid = cx.tid, lane = cx.lane, wave = cx.wave;
  const int wm = wave & 3, wn = wave >> 2;
  const int g = lane >> 4, c = lane & 15, q = (lane >> 2) & 3, pp = lane & 3;

  const char* xbase = (const char*)p.x + (size_t)rb * 256 * 1280;
  unsigned xoff;
  {
    const int row = wave * 8 + (lane >> 3);
    xoff = row * 1280 + (((lane & 7) ^ ((row >> 1) & 7)) << 4);
  }
  const char* wsrc[2];
  unsigned woff[2];
  if (TYPE == 2) {
    const int r = lane / 12, j0 = lane % 12;
    const int kr = 4 * wave + (r & 3);
    const int j = j0 ^ (((kr >> 3) & 1) << 1);
    wsrc[0] = (const char*)p.wg; wsrc[1] = (const char*)p.wu;
    woff[0] = woff[1] = kr * WPITCH + (ct * 96 + j * 8) * 2;
  } else {
    const int r = lane >= 20, j0 = (lane >= 40) ? 0 : lane - 20 * r;
#pragma unroll
    for (int i = 0; i < 2; ++i) {
      const int kr = 2 * (wave + 8 * i) + r;
      const int j = j0 ^ (((kr >> 3) & 1) << 1);
      int col;
      if (TYPE == 1) col = ct * 160 + j * 8;
      else { const int seg = j / 5, jj = j % 5; col = 640 + (seg & 1) * 640 + ct * 80 + (seg >> 1) * 40 + jj * 8; }
      wsrc[i] = (const char*)p.wqkv;
      woff[i] = kr * WPITCH + col * 2;
    }
  }
  auto issue = [&](int op) {
    if ((op >> 8) == 1) {
      const int t = (op & 0xff) >> 2, i = op & 3;
      glds16(xbase + (size_t)(i * 81920 + t * 128), xoff, lbase + (t % 3) * XSLOT + wave * 1024 + i * 8192);
    } else if ((op >> 8) == 2) {
      const int h = (op & 0xff) >> 1, i = op & 1;
      glds16m(wsrc[i] + (size_t)h * 32 * WPITCH, woff[i], lbase + L_W + (h % 5) * WSLOT + wave * (WSLOT / 16) + i * (WSLOT / 2), (1ull << (WSLOT / 256)) - 1);
    } else if ((op >> 8) == 3) touch4(cx.tbase + (size_t)(op & 1) * cx.tstep, cx.toff, lbase + L_SCR);
  };
  const bool gate_wave = (TYPE == 1) && (ct == 0) && (wn == 0);
  u32x2 wph = {0u, 0u};
  if (TYPE == 1) {
    if (wave < 7) {
      unsigned o = wave * 1024 + lane * 16; if (o > 6400 - 16) o = 6400 - 16;
      glds16((const char*)p.wphi + (size_t)ct * 6400, o, lbase + L_RAW + wave * 1024);
    } else {
      glds16((const char*)p.gw, lane * 16, lbase + L_GW);
      glds16m((const char*)p.gw, 1024 + (lane & 15) * 16, lbase + L_GW + 1024, 0xffffull);
    }
  }
  if (TYPE == 0) asm volatile("global_load_dwordx2 %0, %1, %2" : "=v"(wph) : "v"((tid < 400 ? tid : 0) * 8), "s"((const char*)p.wphi + ct * 3200) : "memory");
  static_for<9>([&](auto KC) { constexpr int op = sched_op(-2, decltype(KC)::value); issue(op); });

  int xrd[2];
  {
    const int f = (c >> 1) & 7;
    xrd[0] = (wm * 64 + c) * 128 + ((g ^ f) << 4);
    xrd[1] = (wm * 64 + c) * 128 + (((4 + g) ^ f) << 4);
  }
  int wrd[NT];
  {
    const int sw = (g & 1) << 5;
#pragma unroll
    for (int t = 0; t < NT; ++t) {
      if (TYPE == 2) {
        const int sub = t / 3, tt = wn * 3 + t % 3;
        wrd[t] = L_W + sub * 6144 + (8 * g + q) * 192 + ((tt * 32 + pp * 8) ^ sw);
      } else {
        wrd[t] = L_W + (8 * g + q) * 320 + ((wn * 160 + pp * 40 + t * 8) ^ sw);
      }
    }
  }
  f32x4 acc[4][NT];
#pragma unroll
  for (int m = 0; m < 4; ++m)
#pragma unroll
    for (int t = 0; t < NT; ++t) acc[m][t] = f32x4{0.f, 0.f, 0.f, 0.f};
  f32x4 gacc[4];
#pragma unroll
  for (int m = 0; m < 4; ++m) gacc[m] = f32x4{0.f, 0.f, 0.f, 0.f};
  s16x8 xf[2][4], wf[2][NT];
  auto read_x = [&](int h, int b, int m) { xf[b][m] = *(const s16x8*)(smem + ((h >> 1) % 3) * XSLOT + xrd[h & 1] + m * 2048); };
  auto read_w = [&](int h, int b, int t) {
    const char* a = smem + wrd[t] + (h % 5) * WSLOT;
    wf[b][t] = cat(tr_read(a), tr_read(a + 4 * WP));
  };

  const auto rsP = __builtin_amdgcn_make_buffer_rsrc((void*)(p.ws + WS_PQ), 0, 32 * 2048 * 48 + 32 * 8 * 3360 + 8192, 0x00020000);
  { constexpr int vmn = vm_after(0); WAIT_V(vmn); }
  setup_ready();
  if (TYPE != 2) {
    if (tid < 4) *(unsigned*)(smem + L_ZERO + tid * 4) = 0u;
    if (tid >= 64 && tid < 68) *(unsigned*)(smem + L_ONES + (tid - 64) * 4) = 0x3f803f80u;
  }
  if (TYPE == 0) {
    asm volatile("" : "+v"(wph));
    if (tid < 400) { *(u32x2*)(smem + L_RAW + tid * 8) = wph; *(u32x2*)(smem + L_RAW + 3200 + tid * 8) = wph ^ u32x2{0x80008000u, 0x80008000u}; }
  }
  WAIT_L0(); BAR();
  STAMPK(p.ws, 0, 1); STAMPC(p.ws, 0, 13);
#pragma unroll
  for (int m = 0; m < 4; ++m) read_x(0, 0, m);
#pragma unroll
  for (int t = 0; t < NT; ++t) read_w(0, 0, t);
  static_for<9>([&](auto KC) { constexpr int op = sched_op(-1, decltype(KC)::value); issue(op); });
  static_for<20>([&](auto HC) {
    constexpr int h = decltype(HC)::value, b = h & 1;
    if constexpr (h + 1 < 20) { constexpr int vmn = vm_after(h + 1); WAIT_V(vmn); }
    WAIT_L0(); BAR();
    static_for<4>([&](auto MC) {
      constexpr int m = decltype(MC)::value;
      SCHED();
      if (h + 1 < 20) {
        if (m == 0) { read_w(h + 1, b ^ 1, 0); read_x(h + 1, b ^ 1, 0); read_w(h + 1, b ^ 1, 1); }
        if (m == 1) { read_w(h + 1, b ^ 1, 2); read_w(h + 1, b ^ 1, 3); read_x(h + 1, b ^ 1, 1); }
        if (m == 2) { read_w(h + 1, b ^ 1, 4); if (NT == 6) read_w(h + 1, b ^ 1, NT - 1); read_x(h + 1, b ^ 1, 2); read_x(h + 1, b ^ 1, 3); }
      }
#pragma unroll
      for (int t = 0; t < NT; ++t) acc[m][t] = mfma16(wf[b][t], xf[b][m], acc[m][t]);
      if (h + 1 < 20 && m < 3) {
#pragma unroll
        for (int i = 0; i < NT; ++i) {
          __builtin_amdgcn_sched_group_barrier(0x008, 1, 0);
          __builtin_amdgcn_sched_group_barrier(0x100, 1, 0);
        }
      }
      SCHED();
      constexpr int op0 = sched_op(h, 2 * m), op1 = sched_op(h, 2 * m + 1);
      issue(op0); issue(op1);
    });
    { constexpr int op8 = sched_op(h, 8); issue(op8); }
    if (TYPE == 1 && gate_wave) {
      const s16x8 gf = *(const s16x8*)(smem + L_GW + h * 64 + g * 16);
#pragma unroll
      for (int m = 0; m < 4; ++m) gacc[m] = mfma16(gf, xf[b][m], gacc[m]);
    }
    if constexpr (TYPE == 1 && h >= 14 && h < 18) {
      const u32x4 F = {~0u, ~0u, ~0u, ~0u};
      const int st = rb * 16 + wm * 4 + (h - 14);
      const int o = ((ct * 8 + wn * 4 + g) * 128 + st) * 768 + c * 16;
#pragma unroll
      for (int k = 0; k < 3; ++k) __builtin_amdgcn_raw_buffer_store_b128(F, rsP, o + k * 256, 0, 16);
      if (gate_wave) __builtin_amdgcn_raw_buffer_store_b32(~0u, rsP, g == 0 ? (int)(WS_G - WS_PQ) + (st * 16 + c) * 4 : 0x40000000, 0, 16);
    }
    if constexpr (h == 18) {
      static_assert(vm_after(19) == 0, "drain");
      if (TYPE == 1 && wave == 1) signal();
      early_load();
    }
  });
  STAMPK(p.ws, 0, 2); STAMPC(p.ws, 0, 14);

  const auto rsH = __builtin_amdgcn_make_buffer_rsrc((void*)(p.ws + WS_HA + (size_t)(rb * 4 + wm) * KS_T * 4096), 0, KS_T * 4096, 0x00020000);
  if (TYPE == 2) {
    constexpr float C0 = -2.0f * 0.7978845608028654f * 1.4426950408889634f, C1 = C0 * 0.044715f;
    auto geglu2 = [&](f32x2 xg, f32x2 up) {
      const f32x2 a = xg * __builtin_elementwise_fma(xg * xg, f32x2{C1, C1}, f32x2{C0, C0});
      const f32x2 d = f32x2{fexp2(a[0]), fexp2(a[1])} + f32x2{1.0f, 1.0f};
      const f32x2 h = (xg * up) * f32x2{frcp(d[0]), frcp(d[1])};
      return pack2(h[0], h[1]);
    };
    unsigned t2[4][2];
#pragma unroll
    for (int m = 0; m < 4; ++m) {
      unsigned hp[3][2];
#pragma unroll
      for (int t = 0; t < 3; ++t) {
        hp[t][0] = geglu2(f32x2{acc[m][t][0], acc[m][t][1]}, f32x2{acc[m][t + 3][0], acc[m][t + 3][1]});
        hp[t][1] = geglu2(f32x2{acc[m][t][2], acc[m][t][3]}, f32x2{acc[m][t + 3][2], acc[m][t + 3][3]});
      }
      __builtin_amdgcn_raw_buffer_store_b128(u32x4{hp[0][0], hp[0][1], hp[1][0], hp[1][1]}, rsH, (ct * 3 + wn) * 4096 + m * 1024 + lane * 16, 0, 16);
      t2[m][0] = hp[2][0]; t2[m][1] = hp[2][1];
    }
#pragma unroll
    for (int mp = 0; mp < 2; ++mp) {
      const auto r0 = __builtin_amdgcn_permlane16_swap(t2[2 * mp][0], t2[2 * mp + 1][0], false, false);
      const auto r1 = __builtin_amdgcn_permlane16_swap(t2[2 * mp][1], t2[2 * mp + 1][1], false, false);
      const unsigned a0 = r0[0], b0 = r0[1], a1 = r1[0], b1 = r1[1];
      __builtin_amdgcn_raw_buffer_store_b128(u32x4{a0, a1, b0, b1}, rsH, (ct * 3 + 2) * 4096 + (2 * mp + (g & 1)) * 1024 + ((2 * wn + (g >> 1)) * 16 + c) * 16, 0, 16);
    }
    return;
  }

  s16x8 af[5][3];
  {
    const int src = (TYPE == 1) ? pp : (pp & 1);
    const int hl = (TYPE == 1) ? (wn * 4 + pp) * 800 : (wn * 2 + (pp & 1)) * 800 + (pp >> 1) * 3200;
    const bool on = (g == src);
    const int a0 = on ? L_RAW + hl + q * 40 : L_ZERO;
    const int st = on ? 8 : 0, sd = on ? 160 : 0;
#pragma unroll
    for (int t = 0; t < 5; ++t) {
      af[t][0] = cat(tr_read(smem + a0 + t * st), tr_read(smem + a0 + t * st + sd));
      af[t][1] = cat(tr_read(smem + a0 + t * st + 2 * sd), tr_read(smem + a0 + t * st + 3 * sd));
      af[t][2] = cat(tr_read(smem + a0 + t * st + 4 * sd), tr_read(smem + L_ZERO));
    }
  }
  f32x4 pr[4][5];
#pragma unroll
  for (int m = 0; m < 4; ++m) {
    const u32x4 b0 = {pack2(acc[m][0][0], acc[m][0][1]), pack2(acc[m][0][2], acc[m][0][3]), pack2(acc[m][1][0], acc[m][1][1]), pack2(acc[m][1][2], acc[m][1][3])};
    const u32x4 b1 = {pack2(acc[m][2][0], acc[m][2][1]), pack2(acc[m][2][2], acc[m][2][3]), pack2(acc[m][3][0], acc[m][3][1]), pack2(acc[m][3][2], acc[m][3][3])};
    const unsigned c0 = pack2(acc[m][4][0], acc[m][4][1]), c1 = pack2(acc[m][4][2], acc[m][4][3]);
    const u32x4 b2 = {c0, c1, c0, c1};
#pragma unroll
    for (int t = 0; t < 5; ++t) pr[m][t] = mfma16(af[t][0], __builtin_bit_cast(s16x8, b0), f32x4{0.f, 0.f, 0.f, 0.f});
#pragma unroll
    for (int t = 0; t < 5; ++t) pr[m][t] = mfma16(af[t][1], __builtin_bit_cast(s16x8, b1), pr[m][t]);
#pragma unroll
    for (int t = 0; t < 5; ++t) pr[m][t] = mfma16(af[t][2], __builtin_bit_cast(s16x8, b2), pr[m][t]);
    if (TYPE == 0 && g >= 2) {
      char* vd = smem + (wm * 64 + m * 16 + c) * IMG_PITCH + 320 + (wn * 2 + (g - 2)) * 40;
      *(u32x2*)(vd + 0) = u32x2{b0[0], b0[1]};
      *(u32x2*)(vd + 8) = u32x2{b0[2], b0[3]};
      *(u32x2*)(vd + 16) = u32x2{b1[0], b1[1]};
      *(u32x2*)(vd + 24) = u32x2{b1[2], b1[3]};
      *(u32x2*)(vd + 32) = u32x2{c0, c1};
    }
  }
  if (TYPE == 1) {
#pragma unroll
    for (int m = 0; m < 4; ++m) {
      const int s = rb * 256 + wm * 64 + m * 16 + c;
      const int o = ((ct * 8 + wn * 4 + g) * 128 + (s >> 4)) * 768 + c * 16;
      auto pk = [&](int t, int r) { return nz(pack2(pr[m][t][r], pr[m][t][r + 1])); };
      __builtin_amdgcn_raw_buffer_store_b128(u32x4{pk(0, 0), pk(0, 2), pk(1, 0), pk(1, 2)}, rsP, o, 0, 16);
      __builtin_amdgcn_raw_buffer_store_b128(u32x4{pk(2, 0), 0u, pk(2, 2), pk(3, 0)}, rsP, o + 256, 0, 16);
      __builtin_amdgcn_raw_buffer_store_b128(u32x4{pk(3, 2), pk(4, 0), pk(4, 2), 0u}, rsP, o + 512, 0, 16);
      if (gate_wave) __builtin_amdgcn_raw_buffer_store_b32(nz(__float_as_uint(gacc[m][0])), rsP, g == 0 ? (int)(WS_G - WS_PQ) + s * 4 : 0x40000000, 0, 16);
    }
    return;
  }
#pragma unroll
  for (int m = 0; m < 4; ++m) {
    float ma = max3f(pr[m][0][0], pr[m][0][1], pr[m][0][2]), mb = max3f(pr[m][0][3], pr[m][1][0], pr[m][1][1]);
    ma = max3f(ma, pr[m][1][2], pr[m][1][3]); mb = max3f(mb, pr[m][2][0], pr[m][2][1]);
    ma = max3f(ma, pr[m][2][2], pr[m][2][3]); mb = max3f(mb, pr[m][3][0], pr[m][3][1]);
    ma = max3f(ma, pr[m][3][2], pr[m][3][3]); mb = max3f(mb, pr[m][4][0], pr[m][4][1]);
    ma = max3f(ma, pr[m][4][2], pr[m][4][3]);
    const float mxl = -L2E * __builtin_elementwise_maximum(ma, mb);
    f32x2 e1[10];
#pragma unroll
    for (int t = 0; t < 5; ++t)
#pragma unroll
      for (int r = 0; r < 2; ++r) {
        const f32x2 a = __builtin_elementwise_fma(f32x2{pr[m][t][2 * r], pr[m][t][2 * r + 1]}, f32x2{L2E, L2E}, f32x2{mxl, mxl});
        e1[t * 2 + r] = f32x2{fexp2(a[0]), fexp2(a[1])};
      }
    const f32x2 s2 = (((e1[0] + e1[1]) + (e1[2] + e1[3])) + ((e1[4] + e1[5]) + (e1[6] + e1[7]))) + (e1[8] + e1[9]);
    const float i1 = frcp(s2[0] + s2[1]);
    char* pd = smem + (wm * 64 + m * 16 + c) * IMG_PITCH + (wn * 2 + (g & 1)) * 80 + (g >> 1) * 40;
#pragma unroll
    for (int k = 0; k < 5; ++k) {
      const f32x2 v0 = e1[2 * k] * f32x2{i1, i1}, v1 = e1[2 * k + 1] * f32x2{i1, i1};
      *(u32x2*)(pd + k * 8) = u32x2{pack2(v0[0], v0[1]), pack2(v1[0], v1[1])};
    }
  }
}

DI void kv_stage(const Prm& p, const Ctx& cx, int rb, int ct) {
  char* smem = cx.smem;
  const int lane = cx.lane, wave = cx.wave;
  const int g = lane >> 4, c = lane & 15, q = (lane >> 2) & 3, pp = lane & 3;
  const int hh = wave & 3, u = wave >> 2;
  const int abase = (8 * g + q) * IMG_PITCH + hh * 80;
  const int a0 = abase + pp * 8, a1 = abase + 32 + pp * 8, a2 = abase + 64 + (pp & 1) * 8;
  int b0, bstep;
  if (u == 0) { b0 = (8 * g + q) * IMG_PITCH + 320 + hh * 40 + pp * 8; bstep = IMG_PITCH; }
  else if (pp != 1) { b0 = (8 * g + q) * IMG_PITCH + 320 + hh * 40 + 32; bstep = IMG_PITCH; }
  else { b0 = L_ONES; bstep = 0; }
  f32x4 kv[3];
#pragma unroll
  for (int a = 0; a < 3; ++a) kv[a] = f32x4{0.f, 0.f, 0.f, 0.f};
#pragma unroll
  for (int ks = 0; ks < 8; ++ks) {
    const int ro = ks * 32 * IMG_PITCH;
    const s16x8 bf = cat(tr_read(smem + b0 + ks * 32 * bstep), tr_read(smem + b0 + (ks * 32 + 4) * bstep));
    const s16x8 f0 = cat(tr_read(smem + a0 + ro), tr_read(smem + a0 + ro + 4 * IMG_PITCH));
    const s16x8 f1 = cat(tr_read(smem + a1 + ro), tr_read(smem + a1 + ro + 4 * IMG_PITCH));
    const s16x8 f2 = cat(tr_read(smem + a2 + ro), tr_read(smem + a2 + ro + 4 * IMG_PITCH));
    kv[0] = mfma16(f0, bf, kv[0]);
    kv[1] = mfma16(f1, bf, kv[1]);
    kv[2] = mfma16(f2, bf, kv[2]);
  }
  const auto rsS = __builtin_amdgcn_make_buffer_rsrc((void*)(p.ws + WS_SLAB + (size_t)((ct * 4 + hh) * 8 + rb) * 3360), 0, 3360, 0x00020000);
  const int d = 16 * u + c;
#pragma unroll
  for (int a = 0; a < 3; ++a) {
    const u32x4 w = __builtin_bit_cast(u32x4, kv[a]);
    const int o = (d <= 20 && (a < 2 || g < 2)) ? (d * 40 + 16 * a + 4 * g) * 4 : 0x40000000;
    __builtin_amdgcn_raw_buffer_store_b128(u32x4{nz(w[0]), nz(w[1]), nz(w[2]), nz(w[3])}, rsS, o, 0, 16);
  }
}

__global__ __launch_bounds__(512, 2) void fused(Prm p) {
  __shared__ __attribute__((aligned(1024))) char smem[LDS_BYTES];
  STAMP_DECL;
  const unsigned lbase = lds_addr(smem);
  const int tid = threadIdx.x, lane = tid & 63;
  const int wave = __builtin_amdgcn_readfirstlane(tid >> 6);
  {
    const unsigned long long pc = __builtin_amdgcn_s_getpc() & ~255ull;
    touch4((const void*)pc, tid * 128u, lbase + L_SCR);
  }
  const int bid = blockIdx.x;
  const int xcd = bid & 7, idx = bid >> 3;
  const int rb = (xcd >> 2) * 4 + (idx & 3), slot = (idx >> 2) * 4 + (xcd & 3);
  const int rb64 = xcd * 4 + (idx >> 3), cb = idx & 7, head = idx;
  const bool spare = slot >= 30;
  const int sp = rb * 2 + (slot - 30);
  const int g = lane >> 4, c = lane & 15;
  const unsigned l16 = lane * 16;
  GAS unsigned* flags = (GAS unsigned*)(p.ws + WS_FLAG);
  constexpr float L2E = 1.4426950408889634f;
  STAMPK(p.ws, 0, 0);

  Ctx cx; cx.smem = smem; cx.lbase = lbase; cx.tid = tid; cx.lane = lane; cx.wave = wave;
  {
    const int tarr = wave >> 1;
    cx.tbase = (const char*)(tarr == 0 ? p.x : tarr == 1 ? p.wqkv : tarr == 2 ? p.wg : p.wu);
    const unsigned tshare = tarr == 1 ? 4800u : 4320u;
    cx.tstep = tarr == 0 ? 640u : 256u * tshare;
    const unsigned e = (wave & 1) * 20 + (lane < 20 ? lane : 19);
    unsigned l = e * 128u; if (l > tshare - 4u) l = tshare - 4u;
    cx.toff = (tarr == 0) ? (bid * 8 + e / 5) * 1280u + (e % 5) * 128u : bid * tshare + l;
  }
  auto flag_off = [&]() -> unsigned {
    int ln = lane; asm volatile("" : "+v"(ln));
    unsigned f;
    if (wave == 0 || wave == 4) f = ln < 32 ? FL_S + xcd * 32 + ln : FL_E + (ln & 15);
    else if (wave == 1) f = ln == 0 ? (spare ? FL_E + sp : FL_S + rb * 32 + slot) : FL_D + xcd * 32 + idx;
    else if (spare) f = FL_D + ((wave & 1) + ((wave >> 2) << 1)) * 64 + ln;
    else f = FL_D + rb * 32 + (ln & 31);
    return f;
  };
  unsigned snap;
  { const unsigned fo = flag_off() * 4; asm volatile("global_load_dword %0, %1, %2 sc1" : "=v"(snap) : "v"(fo), "s"(flags) : "memory"); }
  auto early_load = [&]() {
    if (wave == 0) {
      const unsigned fo = flag_off() * 4;
      asm volatile("s_mov_b32 m0, %2\n\ts_nop 0\n\tglobal_load_lds_dword %0, %1 sc1" :: "v"(fo), "s"(flags), "s"(lbase + L_SCR) : "memory");
    }
  };
  auto setup_ready = [&]() {
    asm volatile("" : "+v"(snap));
    if (wave == 1 && lane < 2) { lds_st(smem + C_CUR + lane * 4, snap); lds_st(smem + C_OFF + lane * 4, flag_off() * 4u); }
    if (wave == 2 && lane < 16) lds_st(smem + C_SIG + lane * 4, 0u);
  };
  const auto rsF = __builtin_amdgcn_make_buffer_rsrc((void*)(p.ws + WS_FLAG), 0, 4096, 0x00020000);
  auto bump = [&](int which) {
    if (lane == 0) {
      const unsigned v = lds_add(smem + C_CUR + which * 4, 1u) + 1u;
      __builtin_amdgcn_raw_buffer_store_b32(v, rsF, lds_ld(smem + C_OFF + which * 4), 0, 16);
    }
  };
  auto resignal = [&]() { if (lds_ld(smem + C_SIG)) bump(0); };
  auto signal = [&]() { bump(0); lds_st(smem + C_SIG, 1u); };
  auto wait_changed = [&](unsigned long long mask) {
    const unsigned foff = flag_off();
    for (unsigned spins = 0; spins < SPIN_CAP; ++spins) {
      const unsigned v = gld(flags + foff);
      if ((__ballot(v == snap) & mask) == 0ull) break;
      __builtin_amdgcn_s_sleep(1);
      if ((spins & 63u) == 63u) resignal();
    }
    FENCE();
  };
  constexpr unsigned long long MASK_E = 0xffffull << 32, MASK_Q = 0xf00ull, MASK_G = 0x3ffff000ull;

  if (!spare) {
    if (slot < 8) gemm1_tile<0>(p, cx, rb, slot, early_load, setup_ready, signal STAMP_PASS);
    else if (slot < 12) gemm1_tile<1>(p, cx, rb, slot - 8, early_load, setup_ready, signal STAMP_PASS);
    else gemm1_tile<2>(p, cx, rb, slot - 12, early_load, setup_ready, signal STAMP_PASS);
  } else {
    touch4(cx.tbase, cx.toff, lbase + L_SCR);
    touch4(cx.tbase + cx.tstep, cx.toff, lbase + L_SCR);
    __builtin_amdgcn_s_sleep(SPDELAY);
    const int u0 = sp * 19, u1 = (u0 + 19 < 296) ? u0 + 19 : 296;
    for (int u = u0; u < u0 + 15 && u < u1; ++u) pretile_issue(p, lbase + (u - u0) * 10240, u, wave, lane);
    WAIT_V(14);
    setup_ready();
    WAIT_L0(); BAR();
    for (int u = u0; u < u0 + 4; ++u) pretile_finish(p, smem + (u - u0) * 10240, u, wave, lane);
    WAIT_V(4);
    WAIT_L0(); BAR();
    for (int u = u0 + 15; u < u1; ++u) pretile_issue(p, lbase + (u - u0 - 15) * 10240, u, wave, lane);
    for (int u = u0 + 4; u < u0 + 15 && u < u1; ++u) pretile_finish(p, smem + (u - u0) * 10240, u, wave, lane);
    WAIT_V(7); BAR();
    for (int u = u0 + 15; u < u1; ++u) pretile_finish(p, smem + (u - u0 - 15) * 10240, u, wave, lane);
    {
      const auto rsW = __builtin_amdgcn_make_buffer_rsrc((void*)p.ws, 0, 0x7fffffff, 0x00020000);
      const u32x4 F = {~0u, ~0u, ~0u, ~0u};
      for (int r = 0; r < 2; ++r)
        for (int e = tid; e < KS_O * 4096 / 16; e += 512) __builtin_amdgcn_raw_buffer_store_b128(F, rsW, ((2 * sp + r) * KS_T + KS_H) * 4096 + e * 16, 0, 16);
      for (int e = tid; e < 2 * 8 * 3360 / 16; e += 512) __builtin_amdgcn_raw_buffer_store_b128(F, rsW, (int)WS_SLAB + sp * 2 * 8 * 3360 + e * 16, 0, 16);
    }
    early_load();
  }
  STAMPK(p.ws, 0, 3);
  WAIT_V(0);
  if (spare) {
    WAIT_L0(); BAR();
    if (wave == 1) signal();
  }
  if (wave == 0) {
    if (__ballot(lds_ld(smem + L_SCR + lane * 4) == snap) & (MASK_E | MASK_Q)) wait_changed(MASK_E | MASK_Q);
  }
  WAIT_L0(); BAR();
  if (slot >= 12 && !spare && wave == 1) signal();
  if (slot < 8) { kv_stage(p, cx, rb, slot); WAIT_L0(); BAR(); }
  STAMPK(p.ws, 0, 4);

  const char* Bbase = p.ws + WS_WB + (size_t)cb * KS_T * 5120;
  const auto rsB = __builtin_amdgcn_make_buffer_rsrc((void*)Bbase, 0, KS_T * 5120, 0x00020000);
  const auto rsA = __builtin_amdgcn_make_buffer_rsrc((void*)(p.ws + WS_HA + (size_t)rb64 * KS_T * 4096), 0, KS_T * 4096, 0x00020000);
  auto load_b = [&](int ks, s16x8 (&d)[5]) {
#pragma unroll
    for (int n = 0; n < 5; ++n) d[n] = __builtin_bit_cast(s16x8, __builtin_amdgcn_raw_buffer_load_b128(rsB, l16 + (n & 3) * 1024, ks * 5120 + (n >> 2) * 4096, 16));
  };
  auto dma_bt = [&](int ks, unsigned dst) {
#pragma unroll
    for (int n = 0; n < 5; ++n) glds16_sc1(Bbase + (size_t)ks * 5120 + n * 1024, l16, lbase + dst + n * 1024);
  };
  auto read_bt = [&](int src, s16x8 (&d)[5]) {
#pragma unroll
    for (int n = 0; n < 5; ++n) d[n] = *(const s16x8*)(smem + src + n * 1024 + l16);
  };
  auto load_a = [&](int ks, bool split, s16x8 (&d)[4]) {
#pragma unroll
    for (int m = 0; m < 4; ++m) {
      if (split) {
        const u32x2 lo = __builtin_amdgcn_raw_buffer_load_b64(rsA, lane * 8 + m * 1024, ks * 4096, 16);
        const u32x2 hi = __builtin_amdgcn_raw_buffer_load_b64(rsA, lane * 8 + 512 + m * 1024, ks * 4096, 16);
        d[m] = __builtin_bit_cast(s16x8, u32x4{lo[0], lo[1], hi[0], hi[1]});
      } else {
        d[m] = __builtin_bit_cast(s16x8, __builtin_amdgcn_raw_buffer_load_b128(rsA, l16 + m * 1024, ks * 4096, 16));
      }
    }
  };
  auto missing4 = [&](const s16x8 (&d)[4]) {
    unsigned mx = 0u;
#pragma unroll
    for (int m = 0; m < 4; ++m) {
      const u32x4 w = __builtin_bit_cast(u32x4, d[m]);
      mx = umax(umax(mx, umax(w[0], w[1])), umax(w[2], w[3]));
    }
    return __any(mx == ~0u);
  };
  auto refetch = [&](int ks, bool split, s16x8 (&d)[4]) {
    unsigned spins = 0;
    do {
      __builtin_amdgcn_s_sleep(2);
      if ((++spins & 63u) == 0u) resignal();
      load_a(ks, split, d);
    } while (missing4(d) && spins < SPIN_CAP);
  };
  auto probe = [&](const auto& rs, unsigned off) {
    for (unsigned spins = 0; spins < SPIN_CAP; ++spins) {
      const unsigned v = __builtin_amdgcn_raw_buffer_load_b32(rs, off, 0, 16);
      if (!__any(v == ~0u)) break;
      __builtin_amdgcn_s_sleep(PSL);
      if ((spins & 63u) == 63u) resignal();
    }
    FENCE();
  };
  f32x4 acc[4][5];
#pragma unroll
  for (int m = 0; m < 4; ++m)
#pragma unroll
    for (int n = 0; n < 5; ++n) acc[m][n] = f32x4{0.f, 0.f, 0.f, 0.f};
  auto mma = [&](const s16x8 (&b)[5], const s16x8 (&a)[4]) {
#pragma unroll
    for (int m = 0; m < 4; ++m)
#pragma unroll
      for (int n = 0; n < 5; ++n) acc[m][n] = mfma16(b[n], a[m], acc[m][n]);
  };
  const int wq = wave & 3;
  auto lds_wait = [&](int word, unsigned want) {
    for (unsigned spins = 0; lds_ld(smem + word) < want && spins < SPIN_CAP * 32u; ++spins) __builtin_amdgcn_s_sleep(1);
    FENCE();
  };
  auto arrive = [&](int word) {
    unsigned a = 0;
    if (lane == 0) a = lds_add(smem + word, 1u);
    return (unsigned)__builtin_amdgcn_readfirstlane(a);
  };
  const auto rsP = __builtin_amdgcn_make_buffer_rsrc((void*)(p.ws + WS_PQ), 0, 32 * 2048 * 48 + 32 * 8 * 3360 + 8192, 0x00020000);
  if (wave < 4) {
    s16x8 bw[5][5], av[4][4];
#pragma unroll
    for (int i = 0; i < 5; ++i) load_b(KS_H + wq + 4 * i, bw[i]);
    SCHED();
    unsigned pw[4][5];
    float gv[4];
    for (unsigned spins = 0; spins < SPIN_CAP; ++spins) {
      unsigned mx = 0u;
#pragma unroll
      for (int i = 0; i < 4; ++i) {
        const int st = xcd * 16 + wave * 4 + i;
        const int o = (head * 128 + st) * 768 + c * 16;
        const u32x4 a = __builtin_amdgcn_raw_buffer_load_b128(rsP, o + (g & 1) * 512, 0, 16);
        const u32x4 b = __builtin_amdgcn_raw_buffer_load_b128(rsP, o + 256, 0, 16);
        const unsigned gl = __builtin_amdgcn_raw_buffer_load_b32(rsP, (int)(WS_G - WS_PQ) + (st * 16 + c) * 4, 0, 16);
        pw[i][0] = (g & 1) ? b[2] : a[0]; pw[i][1] = (g & 1) ? b[3] : a[1]; pw[i][2] = (g & 1) ? a[0] : a[2];
        pw[i][3] = (g & 1) ? a[1] : a[3]; pw[i][4] = (g & 1) ? a[2] : b[0];
        gv[i] = __uint_as_float(gl);
        mx = umax(umax(umax(mx, gl), umax(pw[i][0], pw[i][1])), umax(umax(pw[i][2], pw[i][3]), pw[i][4]));
      }
      if (!__any(mx == ~0u)) break;
      __builtin_amdgcn_s_sleep(2);
      if ((spins & 63u) == 63u) resignal();
    }
    STAMPK(p.ws, 0, 5);
    const int e = tid < 210 ? tid : 209;
    u32x4 sv[8];
#pragma unroll
    for (int r = 0; r < 8; ++r) sv[r] = __builtin_amdgcn_raw_buffer_load_b128(rsP, (int)(WS_SLAB - WS_PQ) + (head * 8 + r) * 3360 + e * 16, 0, 16);
    SCHED();
    s16x8 pf[4][2];
    const unsigned sg = (g >= 2) ? 0x80008000u : 0u;
#pragma unroll
    for (int i = 0; i < 4; ++i) {
      f32x2 v[5];
#pragma unroll
      for (int k = 0; k < 5; ++k) { const unsigned w = pw[i][k] ^ sg; v[k] = f32x2{__uint_as_float(w << 16), __uint_as_float(w & 0xffff0000u)}; }
      float mx = max3f(max3f(v[0][0], v[0][1], v[1][0]), max3f(v[1][1], v[2][0], v[2][1]), max3f(max3f(v[3][0], v[3][1], v[4][0]), v[4][1], v[4][1]));
      { const auto r = __builtin_amdgcn_permlane16_swap(__float_as_uint(mx), __float_as_uint(mx), false, false);
        const unsigned r0 = r[0], r1 = r[1]; mx = __builtin_elementwise_maximum(__uint_as_float(r0), __uint_as_float(r1)); }
      const float mxl = -mx * L2E;
      f32x2 s2 = {0.f, 0.f};
#pragma unroll
      for (int k = 0; k < 5; ++k) {
        const f32x2 t = __builtin_elementwise_fma(v[k], f32x2{L2E, L2E}, f32x2{mxl, mxl});
        v[k] = f32x2{fexp2(t[0]), fexp2(t[1])}; s2 += v[k];
      }
      float sm = s2[0] + s2[1];
      { const auto r = __builtin_amdgcn_permlane16_swap(__float_as_uint(sm), __float_as_uint(sm), false, false);
        const unsigned r0 = r[0], r1 = r[1]; sm = __uint_as_float(r0) + __uint_as_float(r1); }
      const float inv = frcp(sm);
      unsigned pk[5];
#pragma unroll
      for (int k = 0; k < 5; ++k) { const f32x2 t = v[k] * f32x2{inv, inv}; pk[k] = pack2(t[0], t[1]); }
      pf[i][0] = __builtin_bit_cast(s16x8, u32x4{pk[0], pk[1], pk[2], pk[3]});
      pf[i][1] = __builtin_bit_cast(s16x8, u32x4{pk[4], 0u, 0u, 0u});
    }
    {
      auto slab_missing = [&]() {
        unsigned mx = 0u;
#pragma unroll
        for (int r = 0; r < 8; ++r) mx = umax(umax(mx, umax(sv[r][0], sv[r][1])), umax(sv[r][2], sv[r][3]));
        return __any(mx == ~0u);
      };
      if (slab_missing()) {
        unsigned spins = 0;
        do {
          ++spins;
          probe(rsP, (unsigned)(WS_SLAB - WS_PQ) + (head * 8 + (lane & 7)) * 3360 + (wave < 3 ? wave * 1024 + 1020 : 3356));
#pragma unroll
          for (int r = 0; r < 8; ++r) sv[r] = __builtin_amdgcn_raw_buffer_load_b128(rsP, (int)(WS_SLAB - WS_PQ) + (head * 8 + r) * 3360 + e * 16, 0, 16);
        } while (slab_missing() && spins < SPIN_CAP);
      }
      f32x4 a = __builtin_bit_cast(f32x4, sv[0]);
#pragma unroll
      for (int r = 1; r < 8; ++r) a += __builtin_bit_cast(f32x4, sv[r]);
      if (tid < 210) {
        const int d = tid / 10, f = (tid % 10) * 4;
        *(unsigned*)(smem + P_KVOP + d * 144 + (16 * (f / 10) + f % 10) * 2) = pack2(a[0], a[1]);
        *(unsigned*)(smem + P_KVOP + d * 144 + (16 * ((f + 2) / 10) + (f + 2) % 10) * 2) = pack2(a[2], a[3]);
      }
      WAIT_L0();
      arrive(C_KV);
      lds_wait(C_KV, 4u);
    }
    STAMPK(p.ws, 0, 6);
    s16x8 kf[3][2];
    {
      const int gp = c >> 2, r = c & 3;
      const int drow[3] = {(gp < 3 ? gp : 2) * 8 + r, (gp & 1) * 8 + 4 + r, 20};
#pragma unroll
      for (int t = 0; t < 3; ++t) {
        kf[t][0] = *(const s16x8*)(smem + P_KVOP + drow[t] * 144 + g * 32);
        kf[t][1] = __builtin_bit_cast(s16x8, u32x4{*(const unsigned*)(smem + P_KVOP + drow[t] * 144 + g * 32 + 16), 0u, 0u, 0u});
      }
    }
    const auto rsO = __builtin_amdgcn_make_buffer_rsrc((void*)(p.ws + WS_HA + (size_t)(xcd * 4 + wave) * KS_T * 4096), 0, KS_T * 4096, 0x00020000);
    const int o16 = (g < 2) ? (KS_H + (head >> 1)) * 4096 + ((2 * (head & 1) + g) * 16 + c) * 16 : 0x40000000;
    const int o8 = (g == 2) ? (KS_H + 16 + (head >> 3)) * 4096 + (head & 1) * 512 + (((head & 7) >> 1) * 16 + c) * 8 : 0x40000000;
#pragma unroll
    for (int i = 0; i < 4; ++i) {
      f32x4 n0 = {0.f, 0.f, 0.f, 0.f}, n1 = n0, dn = n0;
#pragma unroll
      for (int ks = 0; ks < 2; ++ks) {
        n0 = mfma16(kf[0][ks], pf[i][ks], n0);
        n1 = mfma16(kf[1][ks], pf[i][ks], n1);
        dn = mfma16(kf[2][ks], pf[i][ks], dn);
      }
      const float sc = frcp((1.0f + fexp2(-L2E * gv[i])) * (dn[0] + 1e-6f));
      const u32x4 v = {nz(pack2(n0[0] * sc, n0[1] * sc)), nz(pack2(n0[2] * sc, n0[3] * sc)), nz(pack2(n1[0] * sc, n1[1] * sc)), nz(pack2(n1[2] * sc, n1[3] * sc))};
      __builtin_amdgcn_raw_buffer_store_b128(v, rsO, o16 + i * 1024, 0, 16);
      __builtin_amdgcn_raw_buffer_store_b64(u32x2{v[0], v[1]}, rsO, o8 + i * 1024, 0, 16);
    }
    SCHED();
    STAMPK(p.ws, 0, 7);
    if (wave == 0) {
      probe(rsA, (KS_H + 16 + ((lane & 31) >> 3)) * 4096 + 3072 + (lane & 1) * 512 + ((lane & 7) >> 1) * 128 + 120);
      lds_st(smem + C_RQ, 1u);
    } else lds_wait(C_RQ, 1u);
    STAMPK(p.ws, 0, 8);
#pragma unroll
    for (int i = 0; i < 4; ++i) load_a(KS_H + wq + 4 * i, false, av[i]);
    SCHED();
#pragma unroll
    for (int i = 0; i < 4; ++i) {
      if (__builtin_expect(missing4(av[i]), 0)) refetch(KS_H + wq + 4 * i, false, av[i]);
      mma(bw[i], av[i]);
      SCHED();
      if (i == 0) load_a(KS_H + 16 + wq, true, av[0]);
      SCHED();
    }
    if (__builtin_expect(missing4(av[0]), 0)) refetch(KS_H + 16 + wq, true, av[0]);
    mma(bw[4], av[0]);
  } else {
    constexpr int DA = DAH;
    s16x8 bw[4][5], av[DA][4];
    const int myslot = wq * 6 * BS;
#pragma unroll
    for (int i = 0; i < 4; ++i) load_b(wq + 4 * i, bw[i]);
#pragma unroll
    for (int i = 4; i < 10; ++i) dma_bt(wq + 4 * i, myslot + (i - 4) * BS);
    SCHED();
    if (wave == 4) { wait_changed(MASK_G); lds_st(smem + C_RG, 1u); bump(1); } else lds_wait(C_RG, 1u);
    STAMPK(p.ws, 0, 7);
    auto ksof = [&](int i) { const int ks = wq + 4 * i; return ks < KS_H ? ks : KS_H - 1; };
#pragma unroll
    for (int i = 0; i < DA; ++i) { load_a(ksof(i), false, av[i]); SCHED(); }
    float gm[4];
#pragma unroll
    for (int i = 0; i < 14; ++i) {
      if (i >= 4 && i < 10) { read_bt(myslot + (i - 4) * BS, bw[3]); mma(bw[3], av[i % DA]); }
      else if (i < 13 || wq < 2) mma(bw[i < 4 ? i : i - 10], av[i % DA]);
      SCHED();
      if (i + DA < 14) load_a(ksof(i + DA), false, av[i % DA]);
      if (i >= 5 && i < 8) load_b(ksof(i + 5), bw[i - 5]);
      if (i == 9) load_b(ksof(13), bw[3]);
      if (i == 9) {
#pragma unroll
        for (int m = 0; m < 4; ++m) gm[m] = __uint_as_float(__builtin_amdgcn_raw_buffer_load_b32(rsP, (int)(WS_G - WS_PQ) + (rb64 * 64 + m * 16 + c) * 4, 0, 16));
      }
      SCHED();
    }
    STAMPK(p.ws, 0, 8);
    for (unsigned spins = 0; spins < SPIN_CAP; ++spins) {
      if (!__any(umax(umax(__float_as_uint(gm[0]), __float_as_uint(gm[1])), umax(__float_as_uint(gm[2]), __float_as_uint(gm[3]))) == ~0u)) break;
      if ((spins & 63u) == 63u) resignal();
#pragma unroll
      for (int m = 0; m < 4; ++m) gm[m] = __uint_as_float(__builtin_amdgcn_raw_buffer_load_b32(rsP, (int)(WS_G - WS_PQ) + (rb64 * 64 + m * 16 + c) * 4, 0, 16));
    }
#pragma unroll
    for (int m = 0; m < 4; ++m) {
      const float sc = frcp(1.0f + fexp2(L2E * gm[m]));
#pragma unroll
      for (int n = 0; n < 5; ++n) acc[m][n] *= sc;
    }
  }
  STAMPK(p.ws, 0, 9);

  {
    const int park = wq * 6 * BS + l16;
    if (wave >= 4) {
#pragma unroll
      for (int e = 0; e < 20; ++e) *(f32x4*)(smem + park + e * 1024) = acc[e / 5][e % 5];
      WAIT_L0();
      if (lane == 0) lds_st(smem + C_PK + wq * 4, 1u);
    } else {
      lds_wait(C_PK + wq * 4, 1u);
#pragma unroll
      for (int e = 0; e < 20; ++e) *(f32x4*)(smem + park + e * 1024) = acc[e / 5][e % 5] + *(const f32x4*)(smem + park + e * 1024);
    }
    WAIT_L0(); BAR();
    for (int e = wave; e < 20; e += 8) {
      const char* src = smem + e * 1024 + l16;
      const f32x4 r = (*(const f32x4*)(src) + *(const f32x4*)(src + 6 * BS)) + (*(const f32x4*)(src + 12 * BS) + *(const f32x4*)(src + 18 * BS));
      u16* o = p.out + (size_t)(rb64 * 64 + (e / 5) * 16 + c) * D_ + cb * 80 + (e % 5) * 16 + 4 * g;
      *(u32x2*)o = u32x2{pack2(r[0], r[1]), pack2(r[2], r[3])};
    }
  }
  STAMPK(p.ws, 0, 10);
  if (spare) { if (wave & 2) wait_changed(~0ull); }
  else if (slot >= 8 && wave == 2) wait_changed(0xffffffffull);
  STAMPK(p.ws, 0, 12); STAMP_DUMP(p.ws, 0);
}

extern "C" void kernel_launch(void* const* d_in, const int* in_sizes, int n_in,
                              void* d_out, int out_size, void* d_ws, size_t ws_size,
                              hipStream_t stream) {
  Prm p;
  p.x = (const u16*)d_in[0]; p.wqkv = (const u16*)d_in[1]; p.wphi = (const u16*)d_in[2]; p.wo = (const u16*)d_in[3];
  p.wg = (const u16*)d_in[5]; p.wu = (const u16*)d_in[6]; p.wd = (const u16*)d_in[7]; p.gw = (const u16*)d_in[8];
  p.ws = (char*)d_ws; p.out = (u16*)d_out;
  fused<<<256, 512, 0, stream>>>(p);
}
```

```cpp
#include <hip/hip_runtime.h>
#include <hip/hip_bf16.h>
#include <stdint.h>
#include <type_traits>
#include <utility>
#define STAMPK(ws, kern, i) do {} while (0)
#define STAMPC(ws, kern, i) do {} while (0)
#define STAMP_DECL
#define STAMP_DUMP(ws, kern) do {} while (0)
#define STAMP_ARG
#define STAMP_PASS

typedef __attribute__((ext_vector_type(8))) short s16x8;
typedef __attribute__((ext_vector_type(4))) short s16x4;
typedef __attribute__((ext_vector_type(8))) __bf16 bf16x8;
typedef __attribute__((ext_vector_type(2))) __bf16 bf16x2;
typedef __attribute__((ext_vector_type(4))) float f32x4;
typedef __attribute__((ext_vector_type(2))) float f32x2;
typedef __attribute__((ext_vector_type(4))) unsigned u32x4;
typedef __attribute__((ext_vector_type(2))) unsigned u32x2;
typedef __attribute__((ext_vector_type(16))) unsigned u32x16;
typedef unsigned short u16;
#define GAS __attribute__((address_space(1)))
#define LAS __attribute__((address_space(3)))
#define DI __device__ __forceinline__

constexpr int S_ = 2048, D_ = 640, F_ = 1728, NQKV = 1920;
constexpr int KS_H = 54, KS_O = 20, KS_T = 74;

constexpr size_t WS_HA   = 0;
constexpr size_t WS_WB   = WS_HA + (size_t)32 * KS_T * 4096;
constexpr size_t WS_PQ   = WS_WB + (size_t)8 * KS_T * 5120;
constexpr size_t WS_SLAB = WS_PQ + (size_t)32 * 2048 * 48;
constexpr size_t WS_G    = WS_SLAB + (size_t)32 * 8 * 3360;
constexpr size_t WS_MB   = WS_G + 2048 * 4;
constexpr size_t WS_FLAG = WS_MB + 256 * 512;
constexpr int MB_O = 0, MB_K = 32, MB_F = 48, MB_G = 64, MB_P = 84, MB_T = 88;
constexpr int FL_E = 256;
constexpr int FL_D = 512;
static_assert(WS_FLAG % 128 == 0, "flags");
DI f32x4 mfma16(s16x8 a, s16x8 b, f32x4 c) {
  return __builtin_amdgcn_mfma_f32_16x16x32_bf16(__builtin_bit_cast(bf16x8, a), __builtin_bit_cast(bf16x8, b), c, 0, 0, 0);
}
DI f32x4 mfma16h(s16x4 a, s16x4 b, f32x4 c) {
  const s16x4 z = {0, 0, 0, 0};
  return mfma16(__builtin_shufflevector(a, z, 0, 1, 2, 3, 4, 5, 6, 7), __builtin_shufflevector(b, z, 0, 1, 2, 3, 4, 5, 6, 7), c);
}
DI unsigned pack2(float a, float b) {
  bf16x2 v = {(__bf16)a, (__bf16)b};
  return __builtin_bit_cast(unsigned, v);
}
DI s16x4 tr_read(const char* lds) { return __builtin_amdgcn_ds_read_tr16_b64_v4i16((LAS s16x4*)(lds)); }
DI s16x8 cat(s16x4 a, s16x4 b) { return __builtin_shufflevector(a, b, 0, 1, 2, 3, 4, 5, 6, 7); }
DI void glds16(const void* sbase, unsigned voff, unsigned ldsaddr) {
  asm volatile("s_mov_b32 m0, %2\n\ts_nop 0\n\tglobal_load_lds_dwordx4 %0, %1" :: "v"(voff), "s"(sbase), "s"(ldsaddr) : "memory");
}
DI void glds16_sc1(const void* sbase, unsigned voff, unsigned ldsaddr) {
  asm volatile("s_mov_b32 m0, %2\n\ts_nop 0\n\tglobal_load_lds_dwordx4 %0, %1 sc1" :: "v"(voff), "s"(sbase), "s"(ldsaddr) : "memory");
}
DI void glds16m(const void* sbase, unsigned voff, unsigned ldsaddr, unsigned long long mask) {
  asm volatile("s_mov_b64 exec, %3\n\ts_mov_b32 m0, %2\n\ts_nop 0\n\tglobal_load_lds_dwordx4 %0, %1\n\ts_mov_b64 exec, -1"
               :: "v"(voff), "s"(sbase), "s"(ldsaddr), "s"(mask) : "memory");
}
DI void touch4(const void* sbase, unsigned voff, unsigned ldsaddr) {
  asm volatile("s_mov_b32 m0, %2\n\ts_nop 0\n\tglobal_load_lds_dword %0, %1" :: "v"(voff), "s"(sbase), "s"(ldsaddr) : "memory");
}
DI unsigned lds_addr(const char* p) { return (unsigned)(size_t)(const LAS char*)p; }
DI float fexp2(float x) { return __builtin_amdgcn_exp2f(x); }
DI float max3f(float a, float b, float c) { return __builtin_elementwise_maximum(__builtin_elementwise_maximum(a, b), c); }
DI unsigned umax(unsigned a, unsigned b) { return a > b ? a : b; }
DI unsigned nz(unsigned v) { return v < 0xfffffffeu ? v : 0xfffffffeu; }
DI float frcp(float x) { return __builtin_amdgcn_rcpf(x); }
#define WAIT_V(n) asm volatile("s_waitcnt vmcnt(%0)" :: "n"(n) : "memory")
#define WAIT_L0() asm volatile("s_waitcnt lgkmcnt(0)" ::: "memory")
#define BAR() __builtin_amdgcn_s_barrier()
#define SCHED() __builtin_amdgcn_sched_barrier(0)
#define FENCE() asm volatile("" ::: "memory")
template <class F, int... I> DI void static_for_impl(F&& f, std::integer_sequence<int, I...>) { (f(std::integral_constant<int, I>{}), ...); }
template <int N, class F> DI void static_for(F&& f) { static_for_impl(f, std::make_integer_sequence<int, N>{}); }
template <int V> using IC = std::integral_constant<int, V>;

DI unsigned gld(const GAS unsigned* p) { return __hip_atomic_load(p, __ATOMIC_RELAXED, __HIP_MEMORY_SCOPE_AGENT); }
DI void gst(GAS unsigned* p, unsigned v) { __hip_atomic_store(p, v, __ATOMIC_RELAXED, __HIP_MEMORY_SCOPE_AGENT); }
DI unsigned lds_ld(const char* p) { return __hip_atomic_load((const LAS unsigned*)p, __ATOMIC_RELAXED, __HIP_MEMORY_SCOPE_WORKGROUP); }
DI void lds_st(char* p, unsigned v) { __hip_atomic_store((LAS unsigned*)p, v, __ATOMIC_RELAXED, __HIP_MEMORY_SCOPE_WORKGROUP); }
DI void lds_addf(char* p, float v) { LAS float* q = (LAS float*)p; __hip_atomic_fetch_add(q, v, __ATOMIC_RELAXED, __HIP_MEMORY_SCOPE_WORKGROUP); }
DI void lds_or(char* p, unsigned v) { LAS unsigned* q = (LAS unsigned*)p; __hip_atomic_fetch_or(q, v, __ATOMIC_RELAXED, __HIP_MEMORY_SCOPE_WORKGROUP); }
DI unsigned lds_add(char* p, unsigned v) { return __hip_atomic_fetch_add((LAS unsigned*)p, v, __ATOMIC_RELAXED, __HIP_MEMORY_SCOPE_WORKGROUP); }

DI unsigned or16(const u32x16& a) {
  return (((a[0] | a[1]) | (a[2] | a[3])) | ((a[4] | a[5]) | (a[6] | a[7]))) | (((a[8] | a[9]) | (a[10] | a[11])) | ((a[12] | a[13]) | (a[14] | a[15])));
}
template <int W> DI unsigned mb_poll4(const char* mb) {
  u32x4 b;
  asm volatile("s_load_dwordx4 %0, %1, %2 glc\n\ts_waitcnt lgkmcnt(0)" : "=s"(b) : "s"(mb), "n"(W * 4) : "memory");
  return (b[0] | b[1]) | (b[2] | b[3]);
}
template <int W, int W1> DI unsigned mb_poll17(const char* mb) {
  u32x16 a; unsigned b;
  asm volatile("s_load_dwordx16 %0, %2, %3 glc\n\ts_load_dword %1, %2, %4 glc\n\ts_waitcnt lgkmcnt(0)"
               : "=&s"(a), "=&s"(b) : "s"(mb), "n"(W * 4), "n"(W1 * 4) : "memory");
  return or16(a) | b;
}
template <int W> DI unsigned mb_poll18(const char* mb) {
  u32x16 a; u32x2 b;
  asm volatile("s_load_dwordx16 %0, %2, %3 glc\n\ts_load_dwordx2 %1, %2, %4 glc\n\ts_waitcnt lgkmcnt(0)"
               : "=&s"(a), "=&s"(b) : "s"(mb), "n"(W * 4), "n"(W * 4 + 64) : "memory");
  return or16(a) | b[0] | b[1];
}
template <int W> DI unsigned mb_poll32(const char* mb) {
  u32x16 a, b;
  asm volatile("s_load_dwordx16 %0, %2, %3 glc\n\ts_load_dwordx16 %1, %2, %4 glc\n\ts_waitcnt lgkmcnt(0)"
               : "=&s"(a), "=&s"(b) : "s"(mb), "n"(W * 4), "n"(W * 4 + 64) : "memory");
  return or16(a) | or16(b);
}
DI void mb_post(char* ws, int to, int word) {
  const auto rs = __builtin_amdgcn_make_buffer_rsrc((void*)(ws + WS_MB), 0, 256 * 512, 0x00020000);
  __builtin_amdgcn_raw_buffer_store_b32(0u, rs, to >= 0 ? to * 512 + word * 4 : 0x40000000, 0, 16);
}
DI void mb_post8(char* ws, int to, int byte) {
  const auto rs = __builtin_amdgcn_make_buffer_rsrc((void*)(ws + WS_MB), 0, 256 * 512, 0x00020000);
  __builtin_amdgcn_raw_buffer_store_b8((unsigned char)0, rs, to >= 0 ? to * 512 + byte : 0x40000000, 0, 16);
}

struct Prm {
  const u16* x; const u16* wqkv; const u16* wphi; const u16* wo;
  const u16* wg; const u16* wu; const u16* wd; const u16* gw;
  char* ws; u16* out;
};

constexpr int XSLOT = 32768;
constexpr int L_W = 3 * XSLOT;
constexpr int LDS_BYTES = 163840;
constexpr int L_AUX = L_W + 5 * 10240;
constexpr int L_RAW = L_AUX;
constexpr int L_GW = L_RAW + 7168;
constexpr int L_ZERO = L_GW + 2048;
constexpr int L_ONES = L_ZERO + 16;
constexpr int L_CTL = LDS_BYTES - 256;
constexpr int L_EARLY = L_CTL - 512;
constexpr int L_SCR = L_EARLY - 256;
static_assert(L_ONES + 16 <= L_SCR && L_W + 5 * 12288 <= L_SCR, "lds");
constexpr int IMG_PITCH = 488;
constexpr int BS = 5120;
constexpr int KL0 = 16, KLN = 28;
constexpr int P_KVOP = KLN * BS;
constexpr int C_HOLD = L_CTL + 24, C_FIN = L_CTL + 28;
constexpr int C_SIG = L_CTL, C_E1 = L_CTL + 4, C_RQ = L_CTL + 8, C_KV = L_CTL + 12, C_RG = L_CTL + 16, C_STG = L_CTL + 20, C_PK = L_CTL + 32, C_CUR = L_CTL + 64, C_OFF = L_CTL + 160;
constexpr unsigned SPIN_CAP = 1u << 16;

#define CS 16
#define CW 1
constexpr int CL = 576 / (CS * CW);
#define FSL 1
#define AW 4
#define MSL 2
#define BW0 3
#define PP 2
#define HP 1
#define ES 1
#define NB0 8
constexpr int OPX(int t, int i) { return 0x100 | (t << 2) | i; }
constexpr int OPW(int h, int i) { return 0x200 | (h << 1) | i; }
constexpr int OPB(int kb) { return 0x400 | kb; }
constexpr int sched_op(int type, int ph, int k) {
  if (ph >= 15) {
    if (type != 2) return 0;
    const int a[5][9] = {{OPB(35), 0, OPB(43), 0, OPB(51), 0, 0, 0, 0}, {OPB(56), 0, OPB(96), 0, OPB(104), 0, 0, 0, 0},
                         {OPB(64), 0, OPB(72), 0, OPB(80), 0, OPB(88), 0, 0}, {OPB(112), 0, OPB(120), 0, OPB(127), 0, 0, 0, 0},
                         {OPB(0), OPB(8), OPB(16), OPB(24), OPB(27), OPB(132), 0, 0, 0}};
    return a[ph - 15][k];
  }
  if (ph == -2) { const int a[9] = {OPX(0, 0), OPX(0, 1), OPX(0, 2), OPX(0, 3), OPW(0, 0), OPW(0, 1), OPW(1, 0), OPW(1, 1), 0x300}; return k < 9 ? a[k] : 0; }
  if (ph == 1 && k == 8) return 0x301;
  if (ph == -1) { const int a[6] = {OPX(1, 0), OPX(1, 1), OPX(1, 2), OPX(1, 3), OPW(2, 0), OPW(2, 1)}; return k < 6 ? a[k] : 0; }
  if (ph == 0) { const int a[8] = {OPW(3, 0), OPW(3, 1), OPX(2, 0), OPX(2, 1), OPX(2, 2), OPX(2, 3), OPW(4, 0), OPW(4, 1)}; return k < 8 ? a[k] : 0; }
  if (ph == 1) { const int a[8] = {OPW(5, 0), OPW(5, 1), OPW(6, 0), OPW(6, 1), OPX(3, 0), 0, OPX(3, 1), 0}; return k < 8 ? a[k] : 0; }
  if (k >= 8 || (k & 1)) return 0;
  if (k < 4) return (ph + 5 < 20) ? OPW(ph + 5, k >> 1) : 0;
  const int t = (ph & 1) ? (ph + 5) / 2 : (ph + 4) / 2, xh = (ph & 1) ? 0 : 2;
  return t < 10 ? OPX(t, xh + ((k - 4) >> 1)) : 0;
}
constexpr int vm_after(int type, int hn) {
  int n = 0, last = 0;
  const int lastph = (hn == 0) ? -2 : hn - 2;
  for (int ph = -2; ph <= lastph; ++ph)
    for (int k = 0; k < 9; ++k) {
      const int op = sched_op(type, ph, k);
      if (!op) continue;
      ++n;
      if ((op >> 8) == 1 && ((op & 0xff) >> 2) == (hn >> 1)) last = n;
      if ((op >> 8) == 2 && ((op & 0xff) >> 1) == hn) last = n;
    }
  return n - last;
}

constexpr int h_younger(int j) {
  int n = (j < AW) ? 4 * (AW - 1 - j) : 0;
  for (int t = (j < AW ? 0 : j - AW + 1); t < j; ++t) n += (t + 7 < 14 ? 5 : 0) + (t + AW < 14 ? 4 : 0);
  return n;
}
static_assert(AW <= 6, "weights must be older than the h loads one step earlier");
DI int pt_krow(int ks, int slot) {
  const int g = slot >> 3, j = slot & 7;
  if (ks < KS_H) {
    const int G = ks / 3, e = ks % 3;
    return 96 * G + (e < 2 ? 48 * e + 16 * (j >> 2) + 4 * g + (j & 3) : 48 * (g >> 1) + 32 + 8 * (g & 1) + j);
  }
  const int k = ks - KS_H;
  if (k < 16) return (2 * k + (g >> 1)) * 20 + (g & 1) * 8 + j;
  return (8 * (k - 16) + 2 * g + (j >> 2)) * 20 + 16 + (j & 3);
}
DI void pretile_issue(const Prm& p, unsigned ldsbase, int unit, int wave, int lane) {
  const int ks = unit >> 2, q4 = unit & 3;
  const u16* src = (ks < KS_H) ? p.wd : p.wo;
#pragma unroll
  for (int i = 0; i < 2; ++i) {
    const int n = wave + 8 * i;
    const int slot = 2 * n + (lane >= 20), j = lane >= 20 ? lane - 20 : lane;
    const int kr = pt_krow(ks, slot & 31);
    glds16m(src, (unsigned)(kr * D_ + q4 * 160 + (j < 20 ? j : 0) * 8) * 2u, ldsbase + n * 640, (1ull << 40) - 1);
  }
}
template <class U> DI void pretile_finish(const Prm& p, const char* img, int n, U&& unit_of, int wave, int lane) {
  const int g = lane >> 4, q = (lane >> 2) & 3, pp = lane & 3;
  const auto rsW = __builtin_amdgcn_make_buffer_rsrc((void*)(p.ws + WS_WB), 0, 8 * KS_T * 5120, 0x00020000);
  const char* a0 = img + (8 * g + q) * 320 + pp * 8;
#pragma unroll 1
  for (int e = wave; e < n * 10; e += 8) {
    const int j = e / 10, k = e - j * 10, unit = unit_of(j);
    const char* a = a0 + j * 10240 + k * 32;
    const s16x8 v = cat(tr_read(a), tr_read(a + 4 * 320));
    const int cb = 2 * (unit & 3) + (k >= 5), nt = k >= 5 ? k - 5 : k;
    __builtin_amdgcn_raw_buffer_store_b128(__builtin_bit_cast(u32x4, v), rsW, ((cb * KS_T + (unit >> 2)) * 5 + nt) * 1024 + lane * 16, 0, 16);
  }
}

struct Ctx {
  char* smem; unsigned lbase;
  int tid, lane, wave;
  const char* tbase; unsigned toff, tstep;
  const char* bsrc;
};

DI void kv_stage(const Prm& p, const Ctx& cx, int rb, int ct) {
  char* smem = cx.smem;
  const int lane = cx.lane, wave = cx.wave;
  const int g = lane >> 4, c = lane & 15, q = (lane >> 2) & 3, pp = lane & 3;
  const int hh = wave & 3, u = wave >> 2;
  const int abase = (8 * g + q) * IMG_PITCH + hh * 80;
  const int a0 = abase + pp * 8, a1 = abase + 32 + pp * 8, a2 = abase + 64 + (pp & 1) * 8;
  int b0, bstep;
  if (u == 0) { b0 = (8 * g + q) * IMG_PITCH + 320 + hh * 40 + pp * 8; bstep = IMG_PITCH; }
  else if (pp != 1) { b0 = (8 * g + q) * IMG_PITCH + 320 + hh * 40 + 32; bstep = IMG_PITCH; }
  else { b0 = L_ONES; bstep = 0; }
  f32x4 kv[3];
#pragma unroll
  for (int a = 0; a < 3; ++a) kv[a] = f32x4{0.f, 0.f, 0.f, 0.f};
#pragma unroll
  for (int ks = 0; ks < 8; ++ks) {
    const int ro = ks * 32 * IMG_PITCH;
    const s16x8 bf = cat(tr_read(smem + b0 + ks * 32 * bstep), tr_read(smem + b0 + (ks * 32 + 4) * bstep));
    const s16x8 f0 = cat(tr_read(smem + a0 + ro), tr_read(smem + a0 + ro + 4 * IMG_PITCH));
    const s16x8 f1 = cat(tr_read(smem + a1 + ro), tr_read(smem + a1 + ro + 4 * IMG_PITCH));
    const s16x8 f2 = cat(tr_read(smem + a2 + ro), tr_read(smem + a2 + ro + 4 * IMG_PITCH));
    kv[0] = mfma16(f0, bf, kv[0]);
    kv[1] = mfma16(f1, bf, kv[1]);
    kv[2] = mfma16(f2, bf, kv[2]);
  }
  const auto rsS = __builtin_amdgcn_make_buffer_rsrc((void*)(p.ws + WS_SLAB + (size_t)((ct * 4 + hh) * 8 + rb) * 3360), 0, 3360, 0x00020000);
  const int d = 16 * u + c;
#pragma unroll
  for (int a = 0; a < 3; ++a) {
    const u32x4 w = __builtin_bit_cast(u32x4, kv[a]);
    const int o = (d <= 20 && (a < 2 || g < 2)) ? (d * 40 + 16 * a + 4 * g) * 4 : 0x40000000;
    __builtin_amdgcn_raw_buffer_store_b128(u32x4{nz(w[0]), nz(w[1]), nz(w[2]), nz(w[3])}, rsS, o, 0, 16);
  }
  mb_post8(p.ws, lane < 6 ? ct * 32 + 8 + hh * 6 + lane : -1, MB_K * 4 + rb * 2 + u);
}

template <int TYPE, class Early, class Ready, class Turn>
DI void gemm1_tile(const Prm& p, const Ctx& cx, int rb, int ct, Early&& early_load, Ready&& setup_ready, Turn&& turn STAMP_ARG) {
  constexpr int NT = (TYPE == 2) ? 6 : 5;
  constexpr int WSLOT = (TYPE == 2) ? 12288 : 10240;
  constexpr int WP = (TYPE == 2) ? 192 : 320;
  constexpr unsigned WPITCH = (TYPE == 2) ? F_ * 2 : NQKV * 2;
  constexpr float L2E = 1.4426950408889634f;
  char* smem = cx.smem;
  const unsigned lbase = cx.lbase;
  const int tid = cx.tid, lane = cx.lane, wave = cx.wave;
  const int wm = wave & 3, wn = wave >> 2;
  const int g = lane >> 4, c = lane & 15, q = (lane >> 2) & 3, pp = lane & 3;

  const char* xbase = (const char*)p.x + (size_t)rb * 256 * 1280;
  unsigned xoff;
  {
    const int row = wave * 8 + (lane >> 3);
    xoff = row * 1280 + (((lane & 7) ^ ((row >> 1) & 7)) << 4);
  }
  const char* wsrc[2];
  unsigned woff[2];
  if (TYPE == 2) {
    const int r = lane / 12, j0 = lane % 12;
    const int kr = 4 * wave + (r & 3);
    const int j = j0 ^ (((kr >> 3) & 1) << 1);
    wsrc[0] = (const char*)p.wg; wsrc[1] = (const char*)p.wu;
    woff[0] = woff[1] = kr * WPITCH + (ct * 96 + j * 8) * 2;
  } else {
    const int r = lane >= 20, j0 = (lane >= 40) ? 0 : lane - 20 * r;
#pragma unroll
    for (int i = 0; i < 2; ++i) {
      const int kr = 2 * (wave + 8 * i) + r;
      const int j = j0 ^ (((kr >> 3) & 1) << 1);
      int col;
      if (TYPE == 1) col = ct * 160 + j * 8;
      else { const int seg = j / 5, jj = j % 5; col = 640 + (seg & 1) * 640 + ct * 80 + (seg >> 1) * 40 + jj * 8; }
      wsrc[i] = (const char*)p.wqkv;
      woff[i] = kr * WPITCH + col * 2;
    }
  }
  auto issue = [&](int op) {
    if ((op >> 8) == 1) {
      const int t = (op & 0xff) >> 2, i = op & 3;
      glds16(xbase + (size_t)(i * 81920 + t * 128), xoff, lbase + (t % 3) * XSLOT + wave * 1024 + i * 8192);
    } else if ((op >> 8) == 2) {
      const int h = (op & 0xff) >> 1, i = op & 1;
      glds16m(wsrc[i] + (size_t)h * 32 * WPITCH, woff[i], lbase + L_W + (h % 5) * WSLOT + wave * (WSLOT / 16) + i * (WSLOT / 2), (1ull << (WSLOT / 256)) - 1);
    } else if ((op >> 8) == 3) touch4(cx.tbase + (size_t)(op & 1) * cx.tstep, cx.toff, lbase + L_SCR);
    else if ((op >> 8) == 4) glds16_sc1(cx.bsrc + (op & 0xff) * 1024, lane * 16, lbase + (op & 0xff) * 1024 + wave * 1024);
  };
  const bool gate_wave = (TYPE == 1) && (ct == 0) && (wn == 0);
  u32x2 wph = {0u, 0u};
  if (TYPE == 1) {
    if (wave < 7) {
      unsigned o = wave * 1024 + lane * 16; if (o > 6400 - 16) o = 6400 - 16;
      glds16((const char*)p.wphi + (size_t)ct * 6400, o, lbase + L_RAW + wave * 1024);
    } else {
      glds16((const char*)p.gw, lane * 16, lbase + L_GW);
      glds16m((const char*)p.gw, 1024 + (lane & 15) * 16, lbase + L_GW + 1024, 0xffffull);
    }
  }
  if (TYPE == 0) asm volatile("global_load_dwordx2 %0, %1, %2" : "=v"(wph) : "v"((tid < 400 ? tid : 0) * 8), "s"((const char*)p.wphi + ct * 3200) : "memory");
  static_for<9>([&](auto KC) { constexpr int op = sched_op(TYPE, -2, decltype(KC)::value); issue(op); });

  int xrd[2];
  {
    const int f = (c >> 1) & 7;
    xrd[0] = (wm * 64 + c) * 128 + ((g ^ f) << 4);
    xrd[1] = (wm * 64 + c) * 128 + (((4 + g) ^ f) << 4);
  }
  int wrd[NT];
  {
    const int sw = (g & 1) << 5;
#pragma unroll
    for (int t = 0; t < NT; ++t) {
      if (TYPE == 2) {
        const int sub = t / 3, tt = wn * 3 + t % 3;
        wrd[t] = L_W + sub * 6144 + (8 * g + q) * 192 + ((tt * 32 + pp * 8) ^ sw);
      } else {
        wrd[t] = L_W + (8 * g + q) * 320 + ((wn * 160 + pp * 40 + t * 8) ^ sw);
      }
    }
  }
  f32x4 acc[4][NT];
#pragma unroll
  for (int m = 0; m < 4; ++m)
#pragma unroll
    for (int t = 0; t < NT; ++t) acc[m][t] = f32x4{0.f, 0.f, 0.f, 0.f};
  f32x4 gacc[4];
#pragma unroll
  for (int m = 0; m < 4; ++m) gacc[m] = f32x4{0.f, 0.f, 0.f, 0.f};
  s16x8 xf[2][4], wf[2][NT];
  auto read_x = [&](int h, int b, int m) { xf[b][m] = *(const s16x8*)(smem + ((h >> 1) % 3) * XSLOT + xrd[h & 1] + m * 2048); };
  auto read_w = [&](int h, int b, int t) {
    const char* a = smem + wrd[t] + (h % 5) * WSLOT;
    wf[b][t] = cat(tr_read(a), tr_read(a + 4 * WP));
  };

  const auto rsP = __builtin_amdgcn_make_buffer_rsrc((void*)(p.ws + WS_PQ), 0, 32 * 2048 * 48 + 32 * 8 * 3360 + 8192, 0x00020000);
  { constexpr int vmn = vm_after(TYPE, 0); WAIT_V(vmn); }
  setup_ready();
  if (TYPE != 2) {
    if (tid < 4) *(unsigned*)(smem + L_ZERO + tid * 4) = 0u;
    if (tid >= 64 && tid < 68) *(unsigned*)(smem + L_ONES + (tid - 64) * 4) = 0x3f803f80u;
  }
  if (TYPE == 0) {
    asm volatile("" : "+v"(wph));
    if (tid < 400) { *(u32x2*)(smem + L_RAW + tid * 8) = wph; *(u32x2*)(smem + L_RAW + 3200 + tid * 8) = wph ^ u32x2{0x80008000u, 0x80008000u}; }
  }
  WAIT_L0(); BAR();
  STAMPK(p.ws, 0, 1); STAMPC(p.ws, 0, 13);
#pragma unroll
  for (int m = 0; m < 4; ++m) read_x(0, 0, m);
#pragma unroll
  for (int t = 0; t < NT; ++t) read_w(0, 0, t);
  static_for<9>([&](auto KC) { constexpr int op = sched_op(TYPE, -1, decltype(KC)::value); issue(op); });
  static_for<20>([&](auto HC) {
    constexpr int h = decltype(HC)::value, b = h & 1;
    if constexpr (h + 1 < 20) { constexpr int vmn = vm_after(TYPE, h + 1); WAIT_V(vmn); }
    WAIT_L0(); BAR();
    static_for<4>([&](auto MC) {
      constexpr int m = decltype(MC)::value;
      SCHED();
      if (h + 1 < 20) {
        if (m == 0) { read_w(h + 1, b ^ 1, 0); read_x(h + 1, b ^ 1, 0); read_w(h + 1, b ^ 1, 1); }
        if (m == 1) { read_w(h + 1, b ^ 1, 2); read_w(h + 1, b ^ 1, 3); read_x(h + 1, b ^ 1, 1); }
        if (m == 2) { read_w(h + 1, b ^ 1, 4); if (NT == 6) read_w(h + 1, b ^ 1, NT - 1); read_x(h + 1, b ^ 1, 2); read_x(h + 1, b ^ 1, 3); }
      }
#pragma unroll
      for (int t = 0; t < NT; ++t) acc[m][t] = mfma16(wf[b][t], xf[b][m], acc[m][t]);
      if (h + 1 < 20 && m < 3) {
#pragma unroll
        for (int i = 0; i < NT; ++i) {
          __builtin_amdgcn_sched_group_barrier(0x008, 1, 0);
          __builtin_amdgcn_sched_group_barrier(0x100, 1, 0);
        }
      }
      SCHED();
      constexpr int op0 = sched_op(TYPE, h, 2 * m), op1 = sched_op(TYPE, h, 2 * m + 1);
      issue(op0); issue(op1);
    });
    { constexpr int op8 = sched_op(TYPE, h, 8); issue(op8); }
    if (TYPE == 1 && gate_wave) {
      const s16x8 gf = *(const s16x8*)(smem + L_GW + h * 64 + g * 16);
#pragma unroll
      for (int m = 0; m < 4; ++m) gacc[m] = mfma16(gf, xf[b][m], gacc[m]);
    }
    if constexpr (h == (TYPE == 2 ? 10 : 13)) early_load(1);
    if constexpr (h == 18 && TYPE == 2) early_load(0);
  });
  STAMPK(p.ws, 0, 2); STAMPC(p.ws, 0, 14);

  const auto rsH = __builtin_amdgcn_make_buffer_rsrc((void*)(p.ws + WS_HA + (size_t)(rb * 4 + wm) * KS_T * 4096), 0, KS_T * 4096, 0x00020000);
  if (TYPE == 2) {
    constexpr float C0 = -2.0f * 0.7978845608028654f * 1.4426950408889634f, C1 = C0 * 0.044715f;
    auto geglu2 = [&](f32x2 xg, f32x2 up) {
      const f32x2 a = xg * __builtin_elementwise_fma(xg * xg, f32x2{C1, C1}, f32x2{C0, C0});
      const f32x2 d = f32x2{fexp2(a[0]), fexp2(a[1])} + f32x2{1.0f, 1.0f};
      const f32x2 h = (xg * up) * f32x2{frcp(d[0]), frcp(d[1])};
      return pack2(h[0], h[1]);
    };
    unsigned t2[4][2];
#pragma unroll
    for (int m = 0; m < 4; ++m) {
      unsigned hp[3][2];
#pragma unroll
      for (int t = 0; t < 3; ++t) {
        hp[t][0] = geglu2(f32x2{acc[m][t][0], acc[m][t][1]}, f32x2{acc[m][t + 3][0], acc[m][t + 3][1]});
        hp[t][1] = geglu2(f32x2{acc[m][t][2], acc[m][t][3]}, f32x2{acc[m][t + 3][2], acc[m][t + 3][3]});
      }
      __builtin_amdgcn_raw_buffer_store_b128(u32x4{hp[0][0], hp[0][1], hp[1][0], hp[1][1]}, rsH, (ct * 3 + wn) * 4096 + m * 1024 + lane * 16, 0, 16);
      t2[m][0] = hp[2][0]; t2[m][1] = hp[2][1];
    }
#pragma unroll
    for (int mp = 0; mp < 2; ++mp) {
      const auto r0 = __builtin_amdgcn_permlane16_swap(t2[2 * mp][0], t2[2 * mp + 1][0], false, false);
      const auto r1 = __builtin_amdgcn_permlane16_swap(t2[2 * mp][1], t2[2 * mp + 1][1], false, false);
      const unsigned a0 = r0[0], b0 = r0[1], a1 = r1[0], b1 = r1[1];
      __builtin_amdgcn_raw_buffer_store_b128(u32x4{a0, a1, b0, b1}, rsH, (ct * 3 + 2) * 4096 + (2 * mp + (g & 1)) * 1024 + ((2 * wn + (g >> 1)) * 16 + c) * 16, 0, 16);
    }
    return;
  }

  s16x8 af[5][3];
  {
    const int src = (TYPE == 1) ? pp : (pp & 1);
    const int hl = (TYPE == 1) ? (wn * 4 + pp) * 800 : (wn * 2 + (pp & 1)) * 800 + (pp >> 1) * 3200;
    const bool on = (g == src);
    const int a0 = on ? L_RAW + hl + q * 40 : L_ZERO;
    const int st = on ? 8 : 0, sd = on ? 160 : 0;
#pragma unroll
    for (int t = 0; t < 5; ++t) {
      af[t][0] = cat(tr_read(smem + a0 + t * st), tr_read(smem + a0 + t * st + sd));
      af[t][1] = cat(tr_read(smem + a0 + t * st + 2 * sd), tr_read(smem + a0 + t * st + 3 * sd));
      af[t][2] = cat(tr_read(smem + a0 + t * st + 4 * sd), tr_read(smem + L_ZERO));
    }
  }
  f32x4 pr[4][5];
#pragma unroll
  for (int m = 0; m < 4; ++m) {
    const u32x4 b0 = {pack2(acc[m][0][0], acc[m][0][1]), pack2(acc[m][0][2], acc[m][0][3]), pack2(acc[m][1][0], acc[m][1][1]), pack2(acc[m][1][2], acc[m][1][3])};
    const u32x4 b1 = {pack2(acc[m][2][0], acc[m][2][1]), pack2(acc[m][2][2], acc[m][2][3]), pack2(acc[m][3][0], acc[m][3][1]), pack2(acc[m][3][2], acc[m][3][3])};
    const unsigned c0 = pack2(acc[m][4][0], acc[m][4][1]), c1 = pack2(acc[m][4][2], acc[m][4][3]);
    const u32x4 b2 = {c0, c1, c0, c1};
#pragma unroll
    for (int t = 0; t < 5; ++t) pr[m][t] = mfma16(af[t][0], __builtin_bit_cast(s16x8, b0), f32x4{0.f, 0.f, 0.f, 0.f});
#pragma unroll
    for (int t = 0; t < 5; ++t) pr[m][t] = mfma16(af[t][1], __builtin_bit_cast(s16x8, b1), pr[m][t]);
#pragma unroll
    for (int t = 0; t < 5; ++t) pr[m][t] = mfma16(af[t][2], __builtin_bit_cast(s16x8, b2), pr[m][t]);
    if (TYPE == 1) {
      const int s = rb * 256 + wm * 64 + m * 16 + c;
      const int o = ((ct * 8 + wn * 4 + g) * 128 + (s >> 4)) * 640;
      auto pk = [&](int t, int r) { return pack2(pr[m][t][r], pr[m][t][r + 1]); };
      __builtin_amdgcn_raw_buffer_store_b128(u32x4{pk(0, 0), pk(0, 2), pk(1, 0), pk(1, 2)}, rsP, o + c * 16, 0, 16);
      __builtin_amdgcn_raw_buffer_store_b128(u32x4{pk(2, 2), pk(3, 0), pk(3, 2), pk(4, 0)}, rsP, o + 256 + c * 16, 0, 16);
      __builtin_amdgcn_raw_buffer_store_b64(u32x2{pk(2, 0), pk(4, 2)}, rsP, o + 512 + c * 8, 0, 16);
      if (gate_wave) __builtin_amdgcn_raw_buffer_store_b32(__float_as_uint(gacc[m][0]), rsP, g == 0 ? (int)(WS_G - WS_PQ) + s * 4 : 0x40000000, 0, 16);
      SCHED();
    }
    if (TYPE == 0 && g >= 2) {
      char* vd = smem + (wm * 64 + m * 16 + c) * IMG_PITCH + 320 + (wn * 2 + (g - 2)) * 40;
      *(u32x2*)(vd + 0) = u32x2{b0[0], b0[1]};
      *(u32x2*)(vd + 8) = u32x2{b0[2], b0[3]};
      *(u32x2*)(vd + 16) = u32x2{b1[0], b1[1]};
      *(u32x2*)(vd + 24) = u32x2{b1[2], b1[3]};
      *(u32x2*)(vd + 32) = u32x2{c0, c1};
    }
  }
  if (TYPE == 1) {
    early_load(0);
    return;
  }
  u32x4 stg[18];
  const auto rsS = __builtin_amdgcn_make_buffer_rsrc((void*)cx.bsrc, 0, KLN * BS, 0x00020000);
#pragma unroll
  for (int m = 0; m < 4; ++m) {
    SCHED();
    if (m == 2) early_load(0);
#pragma unroll
    for (int r = m * 5; r < m * 5 + 5 && r < 18; ++r) stg[r] = __builtin_amdgcn_raw_buffer_load_b128(rsS, lane * 16, (r < 17 ? r * 8 : 132) * 1024, 16);
    SCHED();
    float ma = max3f(pr[m][0][0], pr[m][0][1], pr[m][0][2]), mb = max3f(pr[m][0][3], pr[m][1][0], pr[m][1][1]);
    ma = max3f(ma, pr[m][1][2], pr[m][1][3]); mb = max3f(mb, pr[m][2][0], pr[m][2][1]);
    ma = max3f(ma, pr[m][2][2], pr[m][2][3]); mb = max3f(mb, pr[m][3][0], pr[m][3][1]);
    ma = max3f(ma, pr[m][3][2], pr[m][3][3]); mb = max3f(mb, pr[m][4][0], pr[m][4][1]);
    ma = max3f(ma, pr[m][4][2], pr[m][4][3]);
    const float mxl = -L2E * __builtin_elementwise_maximum(ma, mb);
    f32x2 e1[10];
#pragma unroll
    for (int t = 0; t < 5; ++t)
#pragma unroll
      for (int r = 0; r < 2; ++r) {
        const f32x2 a = __builtin_elementwise_fma(f32x2{pr[m][t][2 * r], pr[m][t][2 * r + 1]}, f32x2{L2E, L2E}, f32x2{mxl, mxl});
        e1[t * 2 + r] = f32x2{fexp2(a[0]), fexp2(a[1])};
      }
    const f32x2 s2 = (((e1[0] + e1[1]) + (e1[2] + e1[3])) + ((e1[4] + e1[5]) + (e1[6] + e1[7]))) + (e1[8] + e1[9]);
    const float i1 = frcp(s2[0] + s2[1]);
    char* pd = smem + (wm * 64 + m * 16 + c) * IMG_PITCH + (wn * 2 + (g & 1)) * 80 + (g >> 1) * 40;
#pragma unroll
    for (int k = 0; k < 5; ++k) {
      const f32x2 v0 = e1[2 * k] * f32x2{i1, i1}, v1 = e1[2 * k + 1] * f32x2{i1, i1};
      *(u32x2*)(pd + k * 8) = u32x2{pack2(v0[0], v0[1]), pack2(v1[0], v1[1])};
    }
  }
  STAMPK(p.ws, 0, 3);
  WAIT_V(8);
  turn();
  kv_stage(p, cx, rb, ct);
  WAIT_L0(); BAR();
#pragma unroll
  for (int r = 0; r < 18; ++r) *(u32x4*)(smem + ((r < 17 ? r * 8 : 132) + wave) * 1024 + lane * 16) = stg[r];
}

__global__ __launch_bounds__(512, 2) void fused(Prm p) {
  __shared__ __attribute__((aligned(1024))) char smem[LDS_BYTES];
  STAMP_DECL;
  const unsigned lbase = lds_addr(smem);
  const int tid = threadIdx.x, lane = tid & 63;
  const int wave = __builtin_amdgcn_readfirstlane(tid >> 6);
  {
    const unsigned long long pc = __builtin_amdgcn_s_getpc() & ~255ull;
    unsigned len;
    asm volatile(".Lfused_begin:\n\ts_mov_b32 %0, .Lfused_end-.Lfused_begin" : "=s"(len));
    if (wave >= 8 - CW) {
      const unsigned o = ((((blockIdx.x >> 3) & (CS - 1)) * CW + (7 - wave)) * CL + (lane < CL ? lane : 0)) * 128u;
      touch4((const void*)pc, o < len ? o : 0u, lbase + L_SCR);
    }
  }
  const int bid = blockIdx.x;
  const int xcd = bid & 7, idx = bid >> 3;
  const int rb = (xcd >> 2) * 4 + (idx & 3), slot = (idx >> 2) * 4 + (xcd & 3);
  const int rb64 = xcd * 4 + (idx >> 3), cb = idx & 7;
  const bool spare = slot >= 30;
  const int sp = rb * 2 + (slot - 30);
  const int g = lane >> 4, c = lane & 15;
  const unsigned l16 = lane * 16;
  GAS unsigned* flags = (GAS unsigned*)(p.ws + WS_FLAG);
  constexpr float L2E = 1.4426950408889634f;
  STAMPK(p.ws, 0, 0);

  Ctx cx; cx.smem = smem; cx.lbase = lbase; cx.tid = tid; cx.lane = lane; cx.wave = wave;
  {
    const int tarr = wave >> 1;
    cx.tbase = (const char*)(tarr == 0 ? p.x : tarr == 1 ? p.wqkv : tarr == 2 ? p.wg : p.wu);
    const unsigned tshare = tarr == 1 ? 4800u : 4320u;
    cx.tstep = tarr == 0 ? 640u : 256u * tshare;
    const unsigned e = (wave & 1) * 20 + (lane < 20 ? lane : 19);
    unsigned l = e * 128u; if (l > tshare - 4u) l = tshare - 4u;
    cx.toff = (tarr == 0) ? (bid * 8 + e / 5) * 1280u + (e % 5) * 128u : bid * tshare + l;
  }
  cx.bsrc = p.ws + WS_WB + ((size_t)cb * KS_T + KL0) * 5120 + wave * 1024;
  auto flag_off = [&]() -> unsigned {
    int ln = lane; asm volatile("" : "+v"(ln));
    unsigned f;
    if (wave == 1) f = ((ln & 8) || !spare) ? FL_D + xcd * 32 + idx : FL_E + (ln & 7) * 32 + (ln & 16) + sp;
    else if (wave >= 4) f = FL_D + (wave & 3) * 64 + ln;
    else f = FL_E + xcd * 32 + (ln & 31);
    return f;
  };
  unsigned snap;
  { const unsigned fo = flag_off() * 4; asm volatile("global_load_dword %0, %1, %2 sc1" : "=v"(snap) : "v"(fo), "s"(flags) : "memory"); }
  auto early_load = [&](int k) {
    if (wave == 0) {
      const unsigned fo = flag_off() * 4;
      asm volatile("s_mov_b32 m0, %2\n\ts_nop 0\n\tglobal_load_lds_dword %0, %1 sc1" :: "v"(fo), "s"(flags), "s"(lbase + L_EARLY + k * 256) : "memory");
    }
  };
  auto setup_ready = [&]() {
    asm volatile("" : "+v"(snap));
    if (wave == 1 && lane < 24) { lds_st(smem + C_CUR + lane * 4, snap); lds_st(smem + C_OFF + lane * 4, flag_off() * 4u); }
    if (wave == 2 && lane < 16) lds_st(smem + C_SIG + lane * 4, 0u);
  };
  const auto rsF = __builtin_amdgcn_make_buffer_rsrc((void*)(p.ws + WS_FLAG), 0, 4096, 0x00020000);
  auto bump = [&](int which) {
    if (lane < ((which != 1 && spare) ? 8 : 1)) {
      const unsigned v = lds_add(smem + C_CUR + (which * 8 + lane) * 4, 1u) + 1u;
      __builtin_amdgcn_raw_buffer_store_b32(v, rsF, lds_ld(smem + C_OFF + (which * 8 + lane) * 4), 0, 16);
    }
  };
  auto resignal = [&]() { if (lds_ld(smem + C_SIG)) bump(0); };
  auto signal = [&]() { bump(0); lds_st(smem + C_SIG, 1u); };
  auto wait_changed = [&](unsigned long long mask) {
    const unsigned foff = flag_off();
#pragma unroll 1
    for (unsigned spins = 0; spins < SPIN_CAP; ++spins) {
      const unsigned v = gld(flags + foff);
      if ((__ballot(v == snap) & mask) == 0ull) break;
      __builtin_amdgcn_s_sleep(FSL);
      if ((spins & 63u) == 63u) resignal();
    }
    FENCE();
  };
  constexpr unsigned long long MASK_E = 0xffffull;
  const char* mbox = p.ws + WS_MB + (xcd * 32 + idx) * 512;
  auto mb_wait = [&](auto&& poll) {
#pragma unroll 1
    for (unsigned spins = 0; spins < SPIN_CAP && poll() != 0u; ++spins) {
      __builtin_amdgcn_s_sleep(MSL);
      if ((spins & 63u) == 63u) resignal();
    }
  };

  auto turn = [&]() {
    if (wave == 0) {
      if (!spare) lds_st(smem + C_E1, (__ballot(lds_ld(smem + L_EARLY + 256 + lane * 4) == snap) >> 48) ? 0u : 1u);
      if (__ballot(lds_ld(smem + L_EARLY + lane * 4) == snap) & MASK_E) wait_changed(MASK_E);
      bump(1);
    }
    WAIT_L0(); BAR();
  };

  if (!spare) {
    if (slot < 8) gemm1_tile<0>(p, cx, rb, slot, early_load, setup_ready, turn STAMP_PASS);
    else if (slot < 12) gemm1_tile<1>(p, cx, rb, slot - 8, early_load, setup_ready, turn STAMP_PASS);
    else gemm1_tile<2>(p, cx, rb, slot - 12, early_load, setup_ready, turn STAMP_PASS);
  } else {
    auto unit_b = [&](int j) { const int b = sp + 16 * j; return b < 4 * KL0 ? b : b + 4 * KLN; };
    const int nb = sp < 8 ? 12 : 11;
    const auto rsW = __builtin_amdgcn_make_buffer_rsrc((void*)p.ws, 0, 0x7fffffff, 0x00020000);
    const u32x4 F = {~0u, ~0u, ~0u, ~0u};
    auto fill = [&]() {
      __builtin_amdgcn_raw_buffer_store_b128(F, rsW, (int)WS_MB + sp * 8192 + tid * 16, 0, 16);
#pragma unroll
      for (int k = 0; k < 7; ++k) {
        const int e = k * 512 + tid;
        __builtin_amdgcn_raw_buffer_store_b128(F, rsW, (int)WS_SLAB + sp * 2 * 8 * 3360 + (e < 3360 ? e : 3359) * 16, 0, 16);
      }
    };
#pragma unroll 1
    for (int j = 0; j < 7 + NB0; ++j) pretile_issue(p, lbase + j * 10240, j < 7 ? 4 * KL0 + sp * 7 + j : unit_b(j - 7), wave, lane);
    touch4(cx.tbase, cx.toff, lbase + L_SCR);
    touch4(cx.tbase + cx.tstep, cx.toff, lbase + L_SCR);
    WAIT_V(2 + 2 * NB0);
    setup_ready();
    WAIT_L0(); BAR();
    STAMPK(p.ws, 0, 1);
    pretile_finish(p, smem, 7, [&](int j) { return 4 * KL0 + sp * 7 + j; }, wave, lane);
    WAIT_V(0); WAIT_L0(); BAR();
    if (wave == 1) bump(2);
    STAMPK(p.ws, 0, 2);
#pragma unroll 1
    for (int j = NB0; j < nb; ++j) pretile_issue(p, lbase + (j < 8 ? 7 + j : j - 8) * 10240, unit_b(j), wave, lane);
    pretile_finish(p, smem + 7 * 10240, NB0, unit_b, wave, lane);
    fill();
    WAIT_V(8); BAR();
    pretile_finish(p, smem + (7 + NB0) * 10240, 8 - NB0, [&](int j) { return unit_b(j + NB0); }, wave, lane);
    pretile_finish(p, smem, nb - 8, [&](int j) { return unit_b(j + 8); }, wave, lane);
    WAIT_V(0); BAR();
    if (wave == 1) signal();
    STAMPK(p.ws, 0, 11);
    for (int r = 0; r < 2; ++r)
      for (int e = tid; e < KS_O * 4096 / 16; e += 512) __builtin_amdgcn_raw_buffer_store_b128(F, rsW, ((2 * sp + r) * KS_T + KS_H) * 4096 + e * 16, 0, 16);
    early_load(0);
  }
  if (slot >= 8) {
    STAMPK(p.ws, 0, 3);
    WAIT_V(0);
    turn();
    if (spare) { if (wave < 4) mb_post(p.ws, tid, MB_F + sp); }
    else if (slot >= 12) { if (wave == 1) mb_post(p.ws, lane < 32 ? rb * 32 + lane : -1, MB_G + slot - 12); }
    else if (wave == 1 || (wave == 2 && slot == 8)) {
      const int hd = wave == 1 ? (slot - 8) * 8 + (lane & 7) : lane & 31, which = wave == 1 ? lane >> 3 : lane >> 5;
      const int r = (rb & 3) - which;
      mb_post(p.ws, (which < 2 && r >= 0 && r < 3) ? (hd >> 2) * 32 + 8 + (hd & 3) * 6 + (rb >> 2) * 3 + r : -1, MB_P + (wave - 1) * 2 + which);
    } else if (wave == 3 && slot == 8) mb_post(p.ws, lane < 32 ? rb * 32 + lane : -1, MB_T);
  }
  WAIT_L0();
  if (lane == 0) lds_add(smem + C_STG, 1u);
  STAMPK(p.ws, 0, 4);

  const char* Bbase = p.ws + WS_WB + (size_t)cb * KS_T * 5120;
  const auto rsB = __builtin_amdgcn_make_buffer_rsrc((void*)Bbase, 0, KS_T * 5120, 0x00020000);
  const auto rsA = __builtin_amdgcn_make_buffer_rsrc((void*)(p.ws + WS_HA + (size_t)rb64 * KS_T * 4096), 0, KS_T * 4096, 0x00020000);
  auto load_b = [&](int ks, s16x8 (&d)[5]) {
#pragma unroll
    for (int n = 0; n < 5; ++n) d[n] = __builtin_bit_cast(s16x8, __builtin_amdgcn_raw_buffer_load_b128(rsB, l16 + (n & 3) * 1024, ks * 5120 + (n >> 2) * 4096, 16));
  };
  auto dma_bt = [&](int ks, unsigned dst) {
#pragma unroll
    for (int n = 0; n < 5; ++n) glds16_sc1(Bbase + (size_t)ks * 5120 + n * 1024, l16, lbase + dst + n * 1024);
  };
  auto read_bt = [&](int src, s16x8 (&d)[5]) {
#pragma unroll
    for (int n = 0; n < 5; ++n) d[n] = *(const s16x8*)(smem + src + n * 1024 + l16);
  };
  auto load_a = [&](int ks, bool split, s16x8 (&d)[4]) {
#pragma unroll
    for (int m = 0; m < 4; ++m) {
      const int so = ks * 4096 + ((m + wave + 1) & 3) * 1024;
      if (split) {
        const u32x2 lo = __builtin_amdgcn_raw_buffer_load_b64(rsA, lane * 8, so, 16);
        const u32x2 hi = __builtin_amdgcn_raw_buffer_load_b64(rsA, lane * 8 + 512, so, 16);
        d[m] = __builtin_bit_cast(s16x8, u32x4{lo[0], lo[1], hi[0], hi[1]});
      } else {
        d[m] = __builtin_bit_cast(s16x8, __builtin_amdgcn_raw_buffer_load_b128(rsA, l16, so, 16));
      }
    }
  };
  auto missing4 = [&](const s16x8 (&d)[4]) {
    unsigned mx = 0u;
#pragma unroll
    for (int m = 0; m < 4; ++m) {
      const u32x4 w = __builtin_bit_cast(u32x4, d[m]);
      mx = umax(umax(mx, umax(w[0], w[1])), umax(w[2], w[3]));
    }
    return __any(mx == ~0u);
  };
  auto refetch = [&](int ks, bool split, s16x8 (&d)[4]) {
    unsigned spins = 0;
    do {
      __builtin_amdgcn_s_sleep(2);
      if ((++spins & 63u) == 0u) resignal();
      load_a(ks, split, d);
    } while (missing4(d) && spins < SPIN_CAP);
  };
  f32x4 acc[4][5];
#pragma unroll
  for (int m = 0; m < 4; ++m)
#pragma unroll
    for (int n = 0; n < 5; ++n) acc[m][n] = f32x4{0.f, 0.f, 0.f, 0.f};
  auto mma = [&](const s16x8 (&b)[5], const s16x8 (&a)[4]) {
#pragma unroll
    for (int m = 0; m < 4; ++m)
#pragma unroll
      for (int n = 0; n < 5; ++n) acc[m][n] = mfma16(b[n], a[m], acc[m][n]);
  };
  const int wq = wave & 3;
  auto lds_wait = [&](int word, unsigned want) {
#pragma unroll 1
    for (unsigned spins = 0; lds_ld(smem + word) < want && spins < SPIN_CAP * 32u; ++spins) __builtin_amdgcn_s_sleep(1);
    FENCE();
  };
  auto arrive = [&](int word) {
    unsigned a = 0;
    if (lane == 0) a = lds_add(smem + word, 1u);
    return (unsigned)__builtin_amdgcn_readfirstlane(a);
  };
  const auto rsP = __builtin_amdgcn_make_buffer_rsrc((void*)(p.ws + WS_PQ), 0, 32 * 2048 * 48 + 32 * 8 * 3360 + 8192, 0x00020000);
  if (wave < 4) {
    s16x8 bw[5][5], av[4][4];
#pragma unroll
    for (int i = 0; i < BW0; ++i) load_b(KS_H + wq + 4 * i, bw[i]);
    if (idx < 8) {
#pragma unroll
      for (int i = BW0; i < 5; ++i) load_b(KS_H + wq + 4 * i, bw[i]);
      if (lane == 0) lds_add(smem + C_KV, 2u);
    } else {
      const int hd = xcd * 4 + (idx - 8) / 6, part = (idx - 8) % 6;
      const int t0 = part * 64 / 3 + wave, t1 = (part + 1) * 64 / 3;
      const bool six = t0 + 20 < t1;
      if (PP) __builtin_amdgcn_s_setprio(PP);
      mb_wait([&]() { return mb_poll4<MB_P>(mbox); });
      STAMPK(p.ws, 0, 3);
      u32x4 pa[6];
      unsigned pb[6];
      float gv[6];
#pragma unroll
      for (int i = 0; i < 6; ++i) {
        const int st = t0 + ((i < 5 || six) ? 4 * i : 0);
        pa[i] = __builtin_amdgcn_raw_buffer_load_b128(rsP, c * 16 + (g & 1) * 256, (hd * 128 + st) * 640, 16);
        pb[i] = __builtin_amdgcn_raw_buffer_load_b32(rsP, 512 + c * 8 + (g & 1) * 4, (hd * 128 + st) * 640, 16);
        gv[i] = __uint_as_float(__builtin_amdgcn_raw_buffer_load_b32(rsP, c * 4, (int)(WS_G - WS_PQ) + st * 64, 16));
      }
      SCHED();
      u32x4 pf[6];
      unsigned pf4[6];
      const unsigned sg = (g >= 2) ? 0x80000000u : 0u;
      u32x4 kq, sv[8];
      bool early = false;
      int so = (tid < 210 ? tid : 209) * 16;
      const auto rsK = __builtin_amdgcn_make_buffer_rsrc((void*)(p.ws + WS_SLAB + (size_t)hd * 8 * 3360), 0, 8 * 3360, 0x00020000);
      auto load_slabs = [&]() {
#pragma unroll
        for (int r = 0; r < 8; ++r) sv[r] = __builtin_amdgcn_raw_buffer_load_b128(rsK, so, r * 3360, 16);
      };
#pragma unroll
      for (int i = 0; i < 6; ++i) {
        if (i == 2 && ES) asm volatile("s_load_dwordx4 %0, %1, %2 glc" : "=s"(kq) : "s"(mbox), "n"(MB_K * 4) : "memory");
        if (i == 4 && ES) {
          asm volatile("s_waitcnt lgkmcnt(0)" : "+s"(kq), "+v"(pa[4]), "+v"(pb[4]), "+v"(gv[4]), "+v"(pa[5]), "+v"(pb[5]), "+v"(gv[5]) :: "memory");
          early = ((kq[0] | kq[1]) | (kq[2] | kq[3])) == 0u;
          if (early) load_slabs();
        }
        if (i == 4) asm volatile("s_load_dwordx4 %0, %1, %2 glc" : "=s"(kq) : "s"(mbox), "n"(MB_K * 4) : "memory");
        const unsigned pw[5] = {pa[i][0], pa[i][1], pa[i][2], pa[i][3], pb[i]};
        f32x2 v[5];
#pragma unroll
        for (int k = 0; k < 5; ++k) v[k] = f32x2{__uint_as_float((pw[k] << 16) + sg), __uint_as_float((pw[k] & 0xffff0000u) ^ sg)};
        float mx = max3f(max3f(v[0][0], v[0][1], v[1][0]), max3f(v[1][1], v[2][0], v[2][1]), max3f(max3f(v[3][0], v[3][1], v[4][0]), v[4][1], v[4][1]));
        { const auto r = __builtin_amdgcn_permlane16_swap(__float_as_uint(mx), __float_as_uint(mx), false, false);
          const unsigned r0 = r[0], r1 = r[1]; mx = __builtin_elementwise_maximum(__uint_as_float(r0), __uint_as_float(r1)); }
        const float mxl = -mx * L2E;
        f32x2 s2 = {0.f, 0.f};
#pragma unroll
        for (int k = 0; k < 5; ++k) {
          const f32x2 t = __builtin_elementwise_fma(v[k], f32x2{L2E, L2E}, f32x2{mxl, mxl});
          v[k] = f32x2{fexp2(t[0]), fexp2(t[1])}; s2 += v[k];
        }
        float sm = s2[0] + s2[1];
        { const auto r = __builtin_amdgcn_permlane16_swap(__float_as_uint(sm), __float_as_uint(sm), false, false);
          const unsigned r0 = r[0], r1 = r[1]; sm = __uint_as_float(r0) + __uint_as_float(r1); }
        const float inv = frcp(sm);
        unsigned pk[5];
#pragma unroll
        for (int k = 0; k < 5; ++k) { const f32x2 t = v[k] * f32x2{inv, inv}; pk[k] = pack2(t[0], t[1]); }
        pf[i] = u32x4{pk[0], pk[1], pk[2], pk[3]}; pf4[i] = pk[4];
        gv[i] = 1.0f + fexp2(-L2E * gv[i]);
        if (i & 1) {
          asm volatile("" : "+v"(pf[i]), "+v"(pf4[i]), "+v"(gv[i]), "+v"(pf[i - 1]), "+v"(pf4[i - 1]), "+v"(gv[i - 1]));
        }
      }
      STAMPK(p.ws, 0, 5);
      const int d = tid / 10, f = (tid % 10) * 4;
      int w0 = P_KVOP + d * 144 + (16 * (f / 10) + f % 10) * 2, w1 = P_KVOP + d * 144 + (16 * ((f + 2) / 10) + (f + 2) % 10) * 2;
      if (tid >= 210) w0 = w1 = P_KVOP + 3040;
      const int gp = c >> 2, r4 = c & 3;
      int k0 = P_KVOP + ((gp < 3 ? gp : 2) * 8 + r4) * 144 + g * 32, k1 = P_KVOP + ((gp & 1) * 8 + 4 + r4) * 144 + g * 32, k2 = P_KVOP + 20 * 144 + g * 32;
      const int ob = (t0 >> 2) * KS_T * 4096 + (t0 & 3) * 1024;
      int o16 = (g < 2) ? ob + (KS_H + (hd >> 1)) * 4096 + ((2 * (hd & 1) + g) * 16 + c) * 16 : 0x40000000;
      int o8 = (g == 2) ? ob + (KS_H + 16 + (hd >> 3)) * 4096 + (hd & 1) * 512 + (((hd & 7) >> 1) * 16 + c) * 8 : 0x40000000;
      const int ts = t0 - wave, blk = (ts >> 2) + (lane >> 3);
      const bool lo = blk * 4 >= ts, hi = blk * 4 + 4 <= t1;
      const int pb0 = (blk * 8 + (lane & 7)) * 512 + MB_O * 4 + hd * 4;
      int po = (lo && hi) ? pb0 : 0x40000000, ph = (blk * 4 < t1 && lo != hi) ? pb0 + (lo ? 0 : 2) : 0x40000000;
      asm volatile("" : "+v"(w0), "+v"(w1), "+v"(k0), "+v"(k1), "+v"(k2), "+v"(o16), "+v"(o8), "+v"(po), "+v"(ph), "+v"(so));
      const auto rsO = __builtin_amdgcn_make_buffer_rsrc((void*)(p.ws + WS_HA), 0, 32 * KS_T * 4096, 0x00020000);
      const auto rsM = __builtin_amdgcn_make_buffer_rsrc((void*)(p.ws + WS_MB), 0, 256 * 512, 0x00020000);
      lds_wait(C_RQ, 1u);
      asm volatile("s_waitcnt lgkmcnt(0)" : "+s"(kq) :: "memory");
      if ((kq[0] | kq[1]) | (kq[2] | kq[3])) mb_wait([&]() { return mb_poll4<MB_K>(mbox); });
      if (!spare) STAMPK(p.ws, 0, 2);
      for (unsigned spins = 0;;) {
        if (!early) load_slabs();
        early = false;
        unsigned mx = 0u;
#pragma unroll
        for (int r = 0; r < 8; ++r) mx = umax(umax(umax(mx, sv[r][0]), sv[r][1]), umax(sv[r][2], sv[r][3]));
        if (!__any(mx == ~0u) || ++spins >= SPIN_CAP) break;
        __builtin_amdgcn_s_sleep(4);
        if ((spins & 63u) == 63u) resignal();
      }
      {
        auto F4 = [&](int r) { return __builtin_bit_cast(f32x4, sv[r]); };
        const f32x4 a = ((F4(0) + F4(1)) + (F4(2) + F4(3))) + ((F4(4) + F4(5)) + (F4(6) + F4(7)));
        *(unsigned*)(smem + w0) = pack2(a[0], a[1]);
        *(unsigned*)(smem + w1) = pack2(a[2], a[3]);
        FENCE();
        if (lane == 0) lds_add(smem + C_KV, 1u);
        lds_wait(C_KV, 4u);
      }
      STAMPK(p.ws, 0, 6);
      const s16x8 kf0 = *(const s16x8*)(smem + k0), kf1 = *(const s16x8*)(smem + k1), kf2 = *(const s16x8*)(smem + k2);
      auto tail = [&](unsigned v) { return __builtin_bit_cast(s16x8, u32x4{v, 0u, 0u, 0u}); };
      const s16x8 kt0 = tail(*(const unsigned*)(smem + k0 + 16)), kt1 = tail(*(const unsigned*)(smem + k1 + 16)), kt2 = tail(*(const unsigned*)(smem + k2 + 16));
#pragma unroll
      for (int i = 0; i < 6; ++i) {
        if (i < 5 || six) {
          const f32x4 z = {0.f, 0.f, 0.f, 0.f};
          const s16x8 b0 = __builtin_bit_cast(s16x8, pf[i]), b1 = tail(pf4[i]);
          const f32x4 dn = mfma16(kt2, b1, mfma16(kf2, b0, z));
          const f32x4 n0 = mfma16(kt0, b1, mfma16(kf0, b0, z));
          const f32x4 n1 = mfma16(kt1, b1, mfma16(kf1, b0, z));
          const float sc = frcp(gv[i] * (dn[0] + 1e-6f));
          const u32x4 v = {nz(pack2(n0[0] * sc, n0[1] * sc)), nz(pack2(n0[2] * sc, n0[3] * sc)), nz(pack2(n1[0] * sc, n1[1] * sc)), nz(pack2(n1[2] * sc, n1[3] * sc))};
          __builtin_amdgcn_raw_buffer_store_b128(v, rsO, o16 + i * KS_T * 4096, 0, 16);
          __builtin_amdgcn_raw_buffer_store_b64(u32x2{v[0], v[1]}, rsO, o8 + i * KS_T * 4096, 0, 16);
        }
      }
      if (arrive(C_KV) == 7u) {
        __builtin_amdgcn_raw_buffer_store_b32(0u, rsM, po, 0, 16);
        __builtin_amdgcn_raw_buffer_store_b16((unsigned short)0, rsM, ph, 0, 16);
      }
      SCHED();
#pragma unroll
      for (int i = BW0; i < 5; ++i) load_b(KS_H + wq + 4 * i, bw[i]);
    }
    __builtin_amdgcn_s_setprio(0);
    STAMPK(p.ws, 0, 7);
    if (wave == 0) {
      mb_wait([&]() { return mb_poll32<MB_O>(mbox); });
      if (lane == 0) lds_add(smem + C_RQ, 1u);
    }
    lds_wait(C_RQ, 2u);
    STAMPK(p.ws, 0, 8);
#pragma unroll
    for (int i = 0; i < 4; ++i) load_a(KS_H + wq + 4 * i, false, av[i]);
    SCHED();
#pragma unroll
    for (int i = 0; i < 4; ++i) {
      if (__builtin_expect(missing4(av[i]), 0)) refetch(KS_H + wq + 4 * i, false, av[i]);
      mma(bw[i], av[i]);
      SCHED();
      if (i == 0) load_a(KS_H + 16 + wq, true, av[0]);
      SCHED();
    }
    lds_wait(C_PK + wq * 4, 1u);
    if (__builtin_expect(missing4(av[0]), 0)) refetch(KS_H + 16 + wq, true, av[0]);
    mma(bw[4], av[0]);
  } else {
    s16x8 av[AW][4], bl[2][5];
    lds_wait(C_STG, 8u);
    if ((slot >= 8 && slot < 12) || !__builtin_amdgcn_readfirstlane(lds_ld(smem + C_E1))) {
      if (!spare) STAMPK(p.ws, 0, 11);
#pragma unroll
      for (int j = 0; j < 7; ++j) dma_bt(KL0 + wq + 4 * j, (wq + 4 * j) * BS);
    }
    SCHED();
    if (wave == 5) {
      mb_wait([&]() { return mb_poll17<MB_F, MB_T>(mbox); });
      if (lane == 0) lds_add(smem + C_RQ, 1u);
      asm volatile("s_mov_b32 m0, %2\n\ts_nop 0\n\tglobal_load_lds_dword %0, %1 sc1"
                   :: "v"(lane * 4), "s"(p.ws + WS_G + rb64 * 256), "s"(lbase + L_EARLY) : "memory");
    }
    if (wave == 4) { mb_wait([&]() { return mb_poll18<MB_G>(mbox); }); lds_st(smem + C_RG, 1u); } else lds_wait(C_RG, 1u);
    STAMPK(p.ws, 0, 7);
    if (HP) __builtin_amdgcn_s_setprio(HP);
    auto ksof = [&](int j) { const int ks = wq + 4 * (j < 7 ? j + 4 : j < 11 ? j - 7 : j); return ks < KS_H ? ks : KS_H - 1; };
    const char* Abase = p.ws + WS_HA + (size_t)rb64 * KS_T * 4096;
    const unsigned ao[4] = {l16 + ((wq + 1) & 3) * 1024, l16 + ((wq + 2) & 3) * 1024, l16 + ((wq + 3) & 3) * 1024, l16 + wq * 1024};
    auto fetch_a = [&](int j) {
      const char* src = Abase + (size_t)ksof(j) * 4096;
      asm volatile("global_load_dwordx4 %0, %4, %8 sc1\n\tglobal_load_dwordx4 %1, %5, %8 sc1\n\t"
                   "global_load_dwordx4 %2, %6, %8 sc1\n\tglobal_load_dwordx4 %3, %7, %8 sc1"
                   : "=&v"(av[j % AW][0]), "=&v"(av[j % AW][1]), "=&v"(av[j % AW][2]), "=&v"(av[j % AW][3])
                   : "v"(ao[0]), "v"(ao[1]), "v"(ao[2]), "v"(ao[3]), "s"(src) : "memory");
    };
#pragma unroll
    for (int j = 0; j < AW; ++j) fetch_a(j);
    static_for<14>([&](auto JC) {
      constexpr int j = decltype(JC)::value;
      { constexpr int n = h_younger(j); WAIT_V(n); }
      asm volatile("" : "+v"(av[j % AW][0]), "+v"(av[j % AW][1]), "+v"(av[j % AW][2]), "+v"(av[j % AW][3]));
      if (j == 0) read_bt(wq * BS, bl[0]);
      if (j + 1 < 14) read_bt((wq + 4 * ((j + 1) % 7)) * BS, bl[(j + 1) & 1]);
      if (j < 13 || wq < 2) mma(bl[j & 1], av[j % AW]);
      SCHED();
      if (j + 7 < 14) dma_bt(ksof(j + 7), (wq + 4 * j) * BS);
      if (j + AW < 14) fetch_a(j + AW);
      if (j == 9) lds_st(smem + C_PK + wq * 4, 1u);
      SCHED();
    });
    STAMPK(p.ws, 0, 8);
  }
  STAMPK(p.ws, 0, 9);

  {
    char* park = smem + wq * BS + l16 + (wave < 4 ? 0 : 12 * BS);
#pragma unroll
    for (int e = 0; e < 15; ++e) *(f32x4*)(park + (e / 5) * 4 * BS + (e % 5) * 1024) = acc[e / 5][e % 5];
    FENCE();
    const int cnt = wave < 4 ? C_FIN : C_HOLD;
    if (lane == 0) lds_add(smem + cnt, 1u);
    const char* s0 = smem + l16 + (wave < 4 ? 0 : 12 * BS);
    const char* q0 = s0 + ((wq + 3) & 3) * BS, * q1 = s0 + (4 + ((wq + 2) & 3)) * BS, * q2 = s0 + (8 + ((wq + 1) & 3)) * BS;
    char* hs = smem + (24 + wq) * BS + l16;
    if (wave >= 4) {
      lds_wait(C_HOLD, 4u);
      const float sc = frcp(1.0f + fexp2(L2E * *(const float*)(smem + L_EARLY + (wq * 16 + c) * 4)));
#pragma unroll
      for (int n = 0; n < 5; ++n)
        *(f32x4*)(hs + n * 1024) = ((acc[3][n] + *(const f32x4*)(q0 + n * 1024)) + (*(const f32x4*)(q1 + n * 1024) + *(const f32x4*)(q2 + n * 1024))) * sc;
      FENCE();
      if (lane == 0) lds_add(smem + C_FIN, 1u);
    } else {
      u16* o = p.out + (size_t)(rb64 * 64 + wq * 16 + c) * D_ + cb * 80 + 4 * g;
      lds_wait(C_FIN, 8u);
#pragma unroll
      for (int n = 0; n < 5; ++n) {
        const f32x4 r = ((acc[3][n] + *(const f32x4*)(q0 + n * 1024)) + (*(const f32x4*)(q1 + n * 1024) + *(const f32x4*)(q2 + n * 1024))) + *(const f32x4*)(hs + n * 1024);
        *(u32x2*)(o + n * 16) = u32x2{pack2(r[0], r[1]), pack2(r[2], r[3])};
      }
    }
  }
  STAMPK(p.ws, 0, 10);
  if (spare && wave >= 4) wait_changed(~0ull);
  STAMPK(p.ws, 0, 12); STAMP_DUMP(p.ws, 0);
  asm volatile(".Lfused_end:");
}

extern "C" void kernel_launch(void* const* d_in, const int* in_sizes, int n_in,
                              void* d_out, int out_size, void* d_ws, size_t ws_size,
                              hipStream_t stream) {
  Prm p;
  p.x = (const u16*)d_in[0]; p.wqkv = (const u16*)d_in[1]; p.wphi = (const u16*)d_in[2]; p.wo = (const u16*)d_in[3];
  p.wg = (const u16*)d_in[5]; p.wu = (const u16*)d_in[6]; p.wd = (const u16*)d_in[7]; p.gw = (const u16*)d_in[8];
  p.ws = (char*)d_ws; p.out = (u16*)d_out;
  fused<<<256, 512, 0, stream>>>(p);
}
```
